```python
import torch

def _softmax(x, axis=-1):
    return torch.softmax(x, dim=axis)
def _gelu(x):
    return torch.nn.functional.gelu(x, approximate="tanh")
def _silu(x):
    return torch.nn.functional.silu(x)
def _sigmoid(x):
    return torch.sigmoid(x)
def _layernorm(x, axis=-1, eps=1e-5):
    m = x.mean(dim=axis, keepdim=True)
    v = x.var(dim=axis, keepdim=True, unbiased=False)
    return (x - m) / torch.sqrt(v + eps)
def _scatter_add(tgt, idx, src, axis=0):
    return tgt.clone().index_add_(axis, idx, src)
def _segment_sum(src, seg, n):
    out = torch.zeros((n,) + src.shape[1:], dtype=src.dtype)
    return out.index_add_(0, seg, src)
def _pad(x, pw):
    flat = tuple(v for pair in reversed(pw) for v in pair)
    return torch.nn.functional.pad(x, flat)

kernel_name = 'gated_mix__ssm_conv_h3__grouped_kv_mqa__prompt'

def setup_inputs():

    _w = (lambda a: (lambda t: t.to(torch.bfloat16) if t.is_floating_point() else t)(torch.as_tensor(a)))
    def _A_setup():
        B, S, D = 1, 2048, 640
        ks = 4
        return {
            "q": _w((lambda *s: torch.randn(*s, dtype=torch.bfloat16) * 0.05)(B, S, D)),
            "k": _w((lambda *s: torch.randn(*s, dtype=torch.bfloat16) * 0.2)(B, S, D)),
            "v": _w((lambda *s: torch.randn(*s, dtype=torch.bfloat16) * 0.2)(B, S, D)),
            "shift_k": _w((lambda *s: torch.randn(*s, dtype=torch.bfloat16) * 0.2)(D, ks) * 0.5),
            "ssm_k": _w((lambda *s: torch.randn(*s, dtype=torch.bfloat16) * 0.8)(D, S) * (1.0 / S ** 0.5)),
            "ssm_D": _w((lambda *s: torch.randn(*s, dtype=torch.bfloat16) * 0.2)(D)),
        }
    _A_in = _A_setup()

    def _B_setup():
        B, S, D, H = 1, 2048, 640, 32
        Dh = D // H
        return {
            "x": _w((lambda *s: torch.randn(*s, dtype=torch.bfloat16) * 0.05)(B, S, D)),
            "wq": _w((lambda *s: torch.randn(*s, dtype=torch.bfloat16) * 0.8)(D, H * Dh)),
            "wk": _w((lambda *s: torch.randn(*s, dtype=torch.bfloat16) * 0.8)(D, Dh)),
            "wv": _w((lambda *s: torch.randn(*s, dtype=torch.bfloat16) * 0.05)(D, Dh)),
            "wo": _w((lambda *s: torch.randn(*s, dtype=torch.bfloat16) * 0.05)(H * Dh, D)),
        }
    _B_in = _B_setup()

    return {
        "A_q": _A_in["q"],
        "A_k": _A_in["k"],
        "A_v": _A_in["v"],
        "A_shift_k": _A_in["shift_k"],
        "A_ssm_k": _A_in["ssm_k"],
        "A_ssm_D": _A_in["ssm_D"],
        "B_x": _B_in["x"],
        "B_wq": _B_in["wq"],
        "B_wk": _B_in["wk"],
        "B_wv": _B_in["wv"],
        "B_wo": _B_in["wo"],
        "gate_w": _w((lambda *s: torch.randn(*s, dtype=torch.bfloat16) * 0.05)(640) * 16.0),
    }

def reference(A_q, A_k, A_v, A_shift_k, A_ssm_k, A_ssm_D, B_x, B_wq, B_wk, B_wv, B_wo, gate_w):
    def _A_ref(q, k, v, shift_k, ssm_k, ssm_D):
        B, S, D = q.shape
        ks = shift_k.shape[-1]
        k_pad = _pad(k, ((0, 0), (ks - 1, 0), (0, 0)))
        k_shift = torch.zeros_like(k)
        for t in range(ks):
            k_shift = k_shift + k_pad[:, t:t + S, :] * shift_k[:, ks - 1 - t][None, None, :]
        kv = k_shift * v
        L = 2 * S
        kv_pad = _pad(kv, ((0, 0), (0, S), (0, 0)))
        kf_pad = _pad(ssm_k, ((0, 0), (0, S)))
        kv_f = (lambda x, axes=(-2,-1): torch.fft.rfft2(x.float(), dim=axes))(kv_pad[:, None, :, :], axes=(1, 2))
        kf_f = (lambda x, axes=(-2,-1): torch.fft.rfft2(x.float(), dim=axes))(kf_pad[:, None, :], axes=(1, 2))
        kf_f = (lambda x, *d: x.permute(*d))(kf_f, 1, 2, 0)
        y_f = kv_f * kf_f[None, :, :, :]
        y = (lambda x, s, axes=(-2,-1): torch.fft.irfft2(x, s=s, dim=axes).to(torch.bfloat16))(y_f, (1, L), axes=(1, 2))[:, 0, :S, :]
        y = y + kv * ssm_D[None, None, :]
        return q * y

    def _B_ref(x, wq, wk, wv, wo):
        B, S, D = x.shape
        H = 32
        Dh = D // H
        q = (lambda x, *d: x.permute(*d))((x @ wq).reshape(B, S, H, Dh), 0, 2, 1, 3)
        k = x @ wk
        v = x @ wv
        scores = torch.einsum("bhqd,bkd->bhqk", q, k) * (Dh ** -0.5)
        p = _softmax(scores, axis=-1)
        o = torch.einsum("bhqk,bkd->bhqd", p, v)
        return (lambda x, *d: x.permute(*d))(o, 0, 2, 1, 3).reshape(B, S, H * Dh) @ wo

    x = A_q
    g = _sigmoid((x * gate_w).sum(-1, keepdims=True))
    a = _A_ref(x, A_k, A_v, A_shift_k, A_ssm_k, A_ssm_D)
    b = _B_ref(x, B_wq, B_wk, B_wv, B_wo)
    return g * a + (1 - g) * b
```

```cpp
#include <hip/hip_runtime.h>
#include <hip/hip_bf16.h>
#include <stdint.h>

typedef __attribute__((ext_vector_type(8))) __bf16 bf16x8;
typedef __attribute__((ext_vector_type(4))) __bf16 bf16x4;
typedef __attribute__((ext_vector_type(16))) __bf16 bf16x16;
typedef __attribute__((ext_vector_type(4))) short s16x4;
typedef __attribute__((ext_vector_type(16))) float f32x16;
typedef __attribute__((ext_vector_type(8))) float f32x8;
typedef __attribute__((ext_vector_type(4))) float f32x4;
typedef __attribute__((ext_vector_type(4))) unsigned u32x4;
typedef __attribute__((ext_vector_type(2))) unsigned u32x2;
typedef unsigned short u16;
#define LDS_AS __attribute__((address_space(3)))
typedef LDS_AS char* lds_ptr;

constexpr int S = 2048, D = 640;
constexpr float QSCALE = 0.22360679774997896f * 1.4426950408889634f;

constexpr size_t WS_QP = 0;
constexpr size_t WS_O = WS_QP + (size_t)S * D * 2;
constexpr size_t WS_ZT = WS_O + (size_t)S * D * 2;
constexpr size_t WS_UT = WS_ZT + (size_t)S * D * 2;
constexpr size_t WS_KV = WS_UT + (size_t)S * D * 4;
constexpr size_t WS_KN = WS_KV + 64 * 4096;
constexpr size_t WS_G = WS_KN + 1024;

#define SBAR() __builtin_amdgcn_sched_barrier(0)
#define PIN(x) asm volatile("" : "+v"(x))
#define WAIT_VM(N) asm volatile("s_waitcnt vmcnt(" #N ")" ::: "memory")
#define WAIT_BAR(N) asm volatile("s_waitcnt vmcnt(" #N ") lgkmcnt(0)\n\ts_barrier" ::: "memory")
#define BAR_ONLY() asm volatile("s_waitcnt lgkmcnt(0)\n\ts_barrier" ::: "memory")

__device__ __forceinline__ unsigned cvtpk(float lo, float hi) {
  unsigned r;
  asm("v_cvt_pk_bf16_f32 %0, %1, %2" : "=v"(r) : "v"(lo), "v"(hi));
  return r;
}
__device__ __forceinline__ float bf_lo(unsigned w) { return __uint_as_float(w << 16); }
__device__ __forceinline__ float bf_hi(unsigned w) { return __uint_as_float(w & 0xffff0000u); }
__device__ __forceinline__ float rbf(float x) { return bf_lo(cvtpk(x, x)); }

__device__ __forceinline__ void glds16(const void* base, unsigned off, unsigned lds) {
  unsigned sv;
  asm volatile("s_mov_b32 %0, m0\n\ts_mov_b32 m0, %3\n\ts_nop 0\n\tglobal_load_lds_dwordx4 %1, %2\n\ts_mov_b32 m0, %0"
               : "=&s"(sv) : "v"(off), "s"(base), "s"(lds) : "memory");
}
__device__ __forceinline__ void gstore16(void* base, unsigned off, u32x4 v) {
  asm volatile("global_store_dwordx4 %0, %1, %2\n\ts_nop 1" :: "v"(off), "v"(v), "s"(base) : "memory");
}
__device__ __forceinline__ bf16x8 lds_b128(lds_ptr p) { return *(const LDS_AS bf16x8*)p; }
__device__ __forceinline__ s16x4 lds_tr(lds_ptr p) {
  return __builtin_amdgcn_ds_read_tr16_b64_v4i16((LDS_AS s16x4*)p);
}
__device__ __forceinline__ bf16x8 cat4(s16x4 a, s16x4 b) {
  return __builtin_bit_cast(bf16x8, __builtin_shufflevector(a, b, 0, 1, 2, 3, 4, 5, 6, 7));
}
#define MFMA16(a, b, c) __builtin_amdgcn_mfma_f32_16x16x32_bf16(a, b, c, 0, 0, 0)

constexpr int G_XB = 0;
constexpr int G_WB = G_XB + 3 * 16384;
constexpr int G_END = G_WB + 3 * 20480;

struct GemmLane {
  unsigned xoff[4], woff[5];
  unsigned xa[4];
  unsigned wa0, wa1;
};

__device__ __forceinline__ void gemm_setup(GemmLane& g, int w, int l, unsigned lds0) {
#pragma unroll
  for (int i = 0; i < 4; ++i) {
    const int r = 4 * i + (l >> 4);
    g.xoff[i] = (16 * w + r) * 1280 + 16 * ((l & 15) ^ r);
  }
#pragma unroll
  for (int i = 0; i < 5; ++i) {
    const int n = 320 * w + 64 * i + l;
    const int pr = n / 10, cc = n - pr * 10;
    const int k = pr ^ (((pr >> 3) & 1) << 2);
    g.woff[i] = k * 1280 + 16 * cc;
  }
  const int sp = w & 1, nh = w >> 1;
  const int r = l & 15, kq = l >> 4;
#pragma unroll
  for (int ks = 0; ks < 4; ++ks) g.xa[ks] = lds0 + G_XB + (32 * sp + r) * 256 + 16 * ((4 * ks + kq) ^ r);
  const int q = (l & 15) >> 2, p = l & 3;
  g.wa0 = lds0 + G_WB + (8 * kq + 4 * (kq & 1) + q) * 160 + 8 * p + 96 * nh;
  g.wa1 = lds0 + G_WB + (8 * kq + 4 * (1 - (kq & 1)) + q) * 160 + 8 * p + 96 * nh;
}
__device__ __forceinline__ void gemm_issue(const GemmLane& g, const char* abase, const char* wbase, int kc, int buf,
                                           int w, unsigned lds0) {
  const char* ab = abase + kc * 256;
  const char* wb = wbase + (size_t)kc * 128 * 1280;
#pragma unroll
  for (int i = 0; i < 4; ++i) glds16(ab, g.xoff[i], lds0 + G_XB + buf * 16384 + (4 * w + i) * 1024);
#pragma unroll
  for (int i = 0; i < 5; ++i) glds16(wb, g.woff[i], lds0 + G_WB + buf * 20480 + (5 * w + i) * 1024);
}
template <int NH, int EXMODE>
__device__ __forceinline__ void gemm_chunk(const GemmLane& g, f32x4 (&acc)[2][3], int buf, unsigned exa0,
                                           unsigned exa1) {
#pragma unroll
  for (int ks = 0; ks < 4; ++ks) {
    bf16x8 xf[2], wf[3];
#pragma unroll
    for (int a = 0; a < 2; ++a) xf[a] = lds_b128((lds_ptr)(size_t)(g.xa[ks] + buf * 16384 + a * 4096));
    constexpr int NW = (NH == 0) ? 3 : 2;
#pragma unroll
    for (int t = 0; t < NW; ++t)
      wf[t] = cat4(lds_tr((lds_ptr)(size_t)(g.wa0 + buf * 20480 + ks * 5120 + t * 32)),
                   lds_tr((lds_ptr)(size_t)(g.wa1 + buf * 20480 + ks * 5120 + t * 32)));
    if (NH == 1 && EXMODE != 0)
      wf[2] = cat4(lds_tr((lds_ptr)(size_t)(exa0 + ks * 1024)), lds_tr((lds_ptr)(size_t)(exa1 + ks * 1024)));
#pragma unroll
    for (int a = 0; a < 2; ++a) {
#pragma unroll
      for (int t = 0; t < NW; ++t) acc[a][t] = MFMA16(wf[t], xf[a], acc[a][t]);
      if (NH == 1 && EXMODE == 1) acc[a][2] = MFMA16(wf[2], xf[a], acc[a][2]);
      if (NH == 1 && EXMODE == 2) acc[a][2] = MFMA16(xf[a], wf[2], acc[a][2]);
    }
  }
}

constexpr int P_EX = G_END;
constexpr int P_KV = P_EX + 20480;
constexpr int P_GW = P_KV + 4 * 6144;
constexpr int P_SK = P_GW + 2048;
constexpr int P_BYTES = P_SK + 1024;

template <int NH, int EXMODE>
__device__ __forceinline__ void prep_body(const char* x, const char* kin, const char* vin, const char* wq, char* ws,
                                          int qt, int hg, int w, int l, unsigned lds0, const GemmLane& g) {
  const int s0 = qt * 64;
  const char* abase = x + (size_t)s0 * 1280;
  const char* wbase = wq + hg * 160;
  gemm_issue(g, abase, wbase, 0, 0, w, lds0);
  gemm_issue(g, abase, wbase, 1, 1, w, lds0);
  gemm_issue(g, abase, wbase, 2, 2, w, lds0);
  {
    const int sw = s0 + 16 * w;
#pragma unroll
    for (int i = 0; i < 6; ++i) {
      int n = 64 * i + l;
      n = n < 370 ? n : 369;
      const bool isk = n < 200;
      const int m = isk ? n : n - 200;
      const int rr = m / 10, cc = m - rr * 10;
      int row = sw + rr - (isk ? 4 : 1);
      row = row < 0 ? 0 : row;
      const unsigned off = row * 1280 + hg * 160 + cc * 16;
      if (i < 3) glds16(kin, off, lds0 + P_KV + w * 6144 + i * 1024);
      else if (i > 3) glds16(vin, off, lds0 + P_KV + w * 6144 + i * 1024);
      else {
        const char* b = kin;
        const long long dv = (long long)(vin - kin);
        (void)b; (void)dv;
        if (isk) glds16(kin, off, lds0 + P_KV + w * 6144 + i * 1024);
        if (!isk) glds16(vin, off, lds0 + P_KV + w * 6144 + i * 1024);
      }
    }
  }
  f32x4 acc[2][3];
#pragma unroll
  for (int a = 0; a < 2; ++a)
#pragma unroll
    for (int t = 0; t < 3; ++t) acc[a][t] = f32x4{0.f, 0.f, 0.f, 0.f};
  const int kq = l >> 4, q4 = (l & 15) >> 2, p4 = l & 3;
  const unsigned exa0 = lds0 + P_EX + (8 * kq + 4 * (kq & 1) + q4) * 32 + 8 * p4;
  const unsigned exa1 = lds0 + P_EX + (8 * kq + 4 * (1 - (kq & 1)) + q4) * 32 + 8 * p4;
  const int grow = 8 * hg + 2 * w + (l >> 5), gi = l & 31;
  const unsigned gxa = lds0 + G_XB + grow * 256 + 16 * ((gi >> 1) ^ (grow & 15)) + 8 * (gi & 1);
  const unsigned gwa = lds0 + P_GW + gi * 8;
  float gacc = 0.f;
#define GATE(kc, buf)                                                                   \
  {                                                                                     \
    const u32x2 xv = *(const LDS_AS u32x2*)(size_t)(gxa + (buf) * 16384);               \
    const u32x2 wv = *(const LDS_AS u32x2*)(size_t)(gwa + (kc) * 256);                  \
    const unsigned p0 = cvtpk(bf_lo(xv[0]) * bf_lo(wv[0]), bf_hi(xv[0]) * bf_hi(wv[0])); \
    const unsigned p1 = cvtpk(bf_lo(xv[1]) * bf_lo(wv[1]), bf_hi(xv[1]) * bf_hi(wv[1])); \
    gacc += (bf_lo(p0) + bf_hi(p0)) + (bf_lo(p1) + bf_hi(p1));                          \
  }
  WAIT_BAR(25);
  gemm_chunk<NH, EXMODE>(g, acc, 0, exa0, exa1);
  GATE(0, 0);
  WAIT_BAR(16);
  gemm_issue(g, abase, wbase, 3, 0, w, lds0);
  gemm_chunk<NH, EXMODE>(g, acc, 1, exa0 + 4096, exa1 + 4096);
  GATE(1, 1);
  WAIT_BAR(16);
  gemm_issue(g, abase, wbase, 4, 1, w, lds0);
  WAIT_VM(18);
  {
    const int dp = l < 40 ? l : 39;
    const unsigned ka = lds0 + P_KV + w * 6144 + dp * 4;
    const u32x4 sk = *(const LDS_AS u32x4*)(size_t)(lds0 + P_SK + dp * 16);
    const float a0 = bf_lo(sk[0]), a1 = bf_hi(sk[0]), a2 = bf_lo(sk[1]), a3 = bf_hi(sk[1]);
    const float b0 = bf_lo(sk[2]), b1 = bf_hi(sk[2]), b2 = bf_lo(sk[3]), b3 = bf_hi(sk[3]);
    char* ut = ws + WS_UT;
#pragma unroll
    for (int oc = 0; oc < 2; ++oc) {
      const int so = s0 + 16 * w + 8 * oc;
      float kl[12], kh[12];
#pragma unroll
      for (int j = 0; j < 12; ++j) {
        unsigned kk = *(const LDS_AS unsigned*)(size_t)(ka + (8 * oc + j) * 160);
        if (j < 4) kk = (so == 0) ? 0u : kk;
        kl[j] = bf_lo(kk);
        kh[j] = bf_hi(kk);
      }
      float ul[9], uh[9];
#pragma unroll
      for (int j = 0; j < 9; ++j) {
        unsigned vv = *(const LDS_AS unsigned*)(size_t)(ka + 3200 + (8 * oc + j) * 160);
        if (j == 0) vv = (so == 0) ? 0u : vv;
        const float sl = fmaf(kl[j + 3], a0, fmaf(kl[j + 2], a1, fmaf(kl[j + 1], a2, kl[j] * a3)));
        const float sh = fmaf(kh[j + 3], b0, fmaf(kh[j + 2], b1, fmaf(kh[j + 1], b2, kh[j] * b3)));
        ul[j] = sl * bf_lo(vv);
        uh[j] = sh * bf_hi(vv);
      }
      const unsigned pos = (2040 - so) * 2;
      const unsigned ch = (hg * 80 + 2 * dp) * 8192;
      const u32x4 l1 = {cvtpk(ul[8], ul[7]), cvtpk(ul[6], ul[5]), cvtpk(ul[4], ul[3]), cvtpk(ul[2], ul[1])};
      const u32x4 l0 = {cvtpk(ul[7], ul[6]), cvtpk(ul[5], ul[4]), cvtpk(ul[3], ul[2]), cvtpk(ul[1], ul[0])};
      const u32x4 h1 = {cvtpk(uh[8], uh[7]), cvtpk(uh[6], uh[5]), cvtpk(uh[4], uh[3]), cvtpk(uh[2], uh[1])};
      const u32x4 h0 = {cvtpk(uh[7], uh[6]), cvtpk(uh[5], uh[4]), cvtpk(uh[3], uh[2]), cvtpk(uh[1], uh[0])};
      if (l < 40) {
        gstore16(ut, ch + pos, l0);
        gstore16(ut, ch + 4096 + pos, l1);
        gstore16(ut, ch + 8192 + pos, h0);
        gstore16(ut, ch + 8192 + 4096 + pos, h1);
      }
    }
  }
  gemm_chunk<NH, EXMODE>(g, acc, 2, exa0 + 8192, exa1 + 8192);
  GATE(2, 2);
  WAIT_BAR(17);
  gemm_chunk<NH, EXMODE>(g, acc, 0, exa0 + 12288, exa1 + 12288);
  GATE(3, 0);
  WAIT_BAR(8);
  gemm_chunk<NH, EXMODE>(g, acc, 1, exa0 + 16384, exa1 + 16384);
  GATE(4, 1);
#undef GATE
  const int sp = w & 1;
  {
    constexpr int NW = (NH == 0) ? 3 : 2;
#pragma unroll
    for (int a = 0; a < 2; ++a)
#pragma unroll
      for (int t = 0; t < NW; ++t) {
        const int s = s0 + 32 * sp + 16 * a + (l & 15);
        const int n = hg * 80 + 48 * NH + 16 * t + 4 * kq;
        const u32x2 v = {cvtpk(acc[a][t][0] * QSCALE, acc[a][t][1] * QSCALE),
                         cvtpk(acc[a][t][2] * QSCALE, acc[a][t][3] * QSCALE)};
        *(u32x2*)(ws + WS_QP + (size_t)s * 1280 + n * 2) = v;
      }
  }
  {
    float t = gacc;
#pragma unroll
    for (int o = 1; o < 32; o <<= 1) t += __shfl_xor(t, o);
    const float z = rbf(t);
    const float sg = rbf(1.0f / (1.0f + __expf(-z)));
    if (gi == 0) ((float*)(ws + WS_G))[s0 + grow] = sg;
  }
  char* kvt = ws + WS_KV + (size_t)(2 * qt + sp) * 4096;
  if (NH == 1 && EXMODE != 0) {
#pragma unroll
    for (int a = 0; a < 2; ++a) {
      const u32x2 v = {cvtpk(acc[a][2][0], acc[a][2][1]), cvtpk(acc[a][2][2], acc[a][2][3])};
      if (EXMODE == 2) {
        *(u32x2*)(kvt + 1024 + 1024 * a + 16 * ((l & 15) + 32 * (kq >> 1)) + 8 * (kq & 1)) = v;
      } else {
        const int key = 16 * a + (l & 15);
        float nn = bf_lo(v[0]) * bf_lo(v[0]) + bf_hi(v[0]) * bf_hi(v[0]) + bf_lo(v[1]) * bf_lo(v[1]) +
                   bf_hi(v[1]) * bf_hi(v[1]);
        if (hg == 0) {
          *(u32x2*)(kvt + 16 * (key + 32 * (kq >> 1)) + 8 * (kq & 1)) = v;
          nn += __shfl_xor(nn, 16);
          nn += __shfl_xor(nn, 32);
        } else {
          if (kq == 1) *(u32x2*)(kvt + 3072 + 8 * key) = v;
          if (kq == 3) {
            char* vb = kvt + 1024 + 1024 * a + 16 * (16 + 32 * ((key >> 3) & 1)) + 2 * (key & 7);
            *(u16*)(vb) = (u16)(v[0] & 0xffff);
            *(u16*)(vb + 16) = (u16)(v[0] >> 16);
            *(u16*)(vb + 32) = (u16)(v[1] & 0xffff);
            *(u16*)(vb + 48) = (u16)(v[1] >> 16);
          }
          nn = (kq == 1) ? nn : 0.f;
          nn = fmaxf(nn, __shfl_xor(nn, 16));
        }
#pragma unroll
        for (int o = 1; o < 16; o <<= 1) nn = fmaxf(nn, __shfl_xor(nn, o));
        if (l == 0) ((float*)(ws + WS_KN))[(hg == 0 ? 0 : 128) + qt * 4 + 2 * sp + a] = nn;
      }
    }
  }
  if (hg == 3 && NH == 0) {
    if (l < 32) *(u32x2*)(kvt + 3072 + 256 + 8 * l) = u32x2{0x3f803f80u, 0x00003f80u};
    if (l < 48) {
      const int d = 20 + (l % 12), q = l / 12;
      const unsigned c = (d == 20 || d == 24) ? 0x3f803f80u : 0u;
      *(u32x4*)(kvt + 1024 + 1024 * (q >> 1) + 16 * (d + 32 * (q & 1))) = u32x4{c, c, c, c};
    }
  }
}

__global__ void __launch_bounds__(256, 1)
k_prep(const char* __restrict__ x, const char* __restrict__ kin, const char* __restrict__ vin,
       const char* __restrict__ shk, const char* __restrict__ wq, const char* __restrict__ wk,
       const char* __restrict__ wv, const char* __restrict__ gw, char* __restrict__ ws) {
  extern __shared__ __attribute__((aligned(16))) char smem[];
  const unsigned lds0 = (unsigned)(size_t)(lds_ptr)smem;
  const int bid = blockIdx.x;
  const int qt = (bid & 7) * 4 + ((bid >> 3) & 3), hg = bid >> 5;
  const int l = threadIdx.x & 63;
  const int w = __builtin_amdgcn_readfirstlane(threadIdx.x >> 6);
  if (hg < 3) {
#pragma unroll
    for (int i = 0; i < 5; ++i) {
      const int n = 320 * w + 64 * i + l;
      const int pr = n >> 1, cc = n & 1;
      const int k = pr ^ (((pr >> 3) & 1) << 2);
      const unsigned dst = lds0 + P_EX + (5 * w + i) * 1024;
      if (hg == 0) glds16(wk, k * 40 + 16 * cc, dst);
      else if (hg == 1) glds16(wv, k * 40 + 16 * cc, dst);
      else {
        if (cc == 0) glds16(wk, k * 40 + 24, dst);
        if (cc == 1) glds16(wv, k * 40 + 24, dst);
      }
    }
  }
  if (w == 0) glds16(gw, l * 16, lds0 + P_GW);
  if (w == 1 && l < 16) glds16(gw, 1024 + l * 16, lds0 + P_GW + 1024);
  if (w == 2 && l < 40) glds16(shk, hg * 640 + l * 16, lds0 + P_SK);
  GemmLane g;
  gemm_setup(g, w, l, lds0);
  if (w < 2) prep_body<0, 0>(x, kin, vin, wq, ws, qt, hg, w, l, lds0, g);
  else if (hg == 1) prep_body<1, 2>(x, kin, vin, wq, ws, qt, hg, w, l, lds0, g);
  else if (hg < 3) prep_body<1, 1>(x, kin, vin, wq, ws, qt, hg, w, l, lds0, g);
  else prep_body<1, 0>(x, kin, vin, wq, ws, qt, hg, w, l, lds0, g);
}

constexpr int M_RING = 0;
constexpr int CV_H = 4160, CV_U = 4640, CV_CH = 4 * CV_H + 2 * CV_U;
constexpr int M_CV = 65536;
constexpr int M_BYTES = M_CV + 3 * CV_CH;

#define MFMA32(a, b, c) __builtin_amdgcn_mfma_f32_32x32x16_bf16(a, b, c, 0, 0, 0)
#define MFMA8(a, b, c) __builtin_amdgcn_mfma_f32_32x32x8bf16_1k(a, b, c, 0, 0, 0)
__device__ __forceinline__ f32x16 pv_lo(bf16x8 v, f32x16 p, f32x16 o, int idx) {
  const f32x8 h = __builtin_shufflevector(p, p, 0, 1, 2, 3, 4, 5, 6, 7);
  return __builtin_amdgcn_smfmac_f32_32x32x32_bf16(v, __builtin_bit_cast(bf16x16, h), o, idx, 0, 0);
}
__device__ __forceinline__ f32x16 pv_hi(bf16x8 v, f32x16 p, f32x16 o, int idx) {
  const f32x8 h = __builtin_shufflevector(p, p, 8, 9, 10, 11, 12, 13, 14, 15);
  return __builtin_amdgcn_smfmac_f32_32x32x32_bf16(v, __builtin_bit_cast(bf16x16, h), o, idx, 0, 0);
}
#define EX2(v) __builtin_amdgcn_exp2f(v)

__device__ __forceinline__ void attn_wave(char* ws, int qt, int hg, int w, int l, unsigned lds0) {
  const char* kvg = ws + WS_KV;
  const int h = l >> 5;
  const int head = hg * 4 + (w & 3);
  const int srow = qt * 64 + 32 * (w >> 2) + (l & 31);
  const unsigned dsrc = w * 2048 + l * 16;
  const unsigned ddst = lds0 + M_RING + w * 2048;
#define RING_ISSUE(c)                                                            \
  {                                                                              \
    const char* b_ = kvg + ((c) & 15) * 16384;                                   \
    glds16(b_, dsrc, ddst + ((c) & 3) * 16384);                                  \
    glds16(b_, dsrc + 1024, ddst + ((c) & 3) * 16384 + 1024);                    \
  }
  const char* qrow = ws + WS_QP + (size_t)srow * 1280 + head * 40;
  u32x4 q16 = *(const u32x4*)(qrow + 16 * h);
  u32x2 q8 = *(const u32x2*)(qrow + 32);
  const f32x4 kn4 = *(const f32x4*)(ws + WS_KN + (h * 128 + (l & 31) * 4) * 4);
  RING_ISSUE(0);
  RING_ISSUE(1);
  RING_ISSUE(2);
  float kn = fmaxf(fmaxf(kn4[0], kn4[1]), fmaxf(kn4[2], kn4[3]));
#pragma unroll
  for (int o = 1; o < 32; o <<= 1) kn = fmaxf(kn, __shfl_xor(kn, o));
  kn += __shfl_xor(kn, 32);
  float qn = 0.f;
#pragma unroll
  for (int i = 0; i < 4; ++i) qn += bf_lo(q16[i]) * bf_lo(q16[i]) + bf_hi(q16[i]) * bf_hi(q16[i]);
  if (h == 0) {
#pragma unroll
    for (int i = 0; i < 2; ++i) qn += bf_lo(q8[i]) * bf_lo(q8[i]) + bf_hi(q8[i]) * bf_hi(q8[i]);
  }
  qn += __shfl_xor(qn, 32);
  {
    const float mref = sqrtf(qn * kn) * 1.01f - 64.0f;
    const unsigned nb = cvtpk(-mref, 0.f);
    if (h == 1) q8 = u32x2{nb, 0u};
  }
  const bf16x8 qa = __builtin_bit_cast(bf16x8, q16);
  s16x4 qb = __builtin_bit_cast(s16x4, q8);
  const int idx = 0xdddd;
  const unsigned ra = lds0 + M_RING + l * 16, rb = lds0 + M_RING + 3072 + l * 8;
#define LK16(c, t) lds_b128((lds_ptr)(size_t)(ra + (c) + (t) * 4096))
#define LK8(c, t) (*(const LDS_AS s16x4*)(size_t)(rb + (c) + (t) * 4096))
#define LV0(c, t) lds_b128((lds_ptr)(size_t)(ra + (c) + (t) * 4096 + 1024))
#define LV1(c, t) lds_b128((lds_ptr)(size_t)(ra + (c) + (t) * 4096 + 2048))
  const f32x16 zero = {0.f, 0.f, 0.f, 0.f, 0.f, 0.f, 0.f, 0.f, 0.f, 0.f, 0.f, 0.f, 0.f, 0.f, 0.f, 0.f};
  f32x16 O = zero, SA, SB;
  WAIT_BAR(4);
  bf16x8 k16, v0, v1;
  s16x4 k8;
  k16 = LK16(0, 0);
  k8 = LK8(0, 0);
  SA = MFMA32(k16, qa, zero);
  SA = MFMA8(k8, qb, SA);
  k16 = LK16(0, 1);
  k8 = LK8(0, 1);
  v0 = LV0(0, 0);
  v1 = LV1(0, 0);
#define E4(X, i)                                                                           \
  X[i] = EX2(X[i]); X[i + 1] = EX2(X[i + 1]); X[i + 2] = EX2(X[i + 2]); X[i + 3] = EX2(X[i + 3]); \
  PIN(X); SBAR();
#define STEP(SC, SN, ck, tk, cv, tv)              \
  {                                               \
    SN = MFMA32(k16, qa, zero);                   \
    k16 = LK16(ck, tk);                           \
    E4(SC, 0);                                    \
    SN = MFMA8(k8, qb, SN);                       \
    k8 = LK8(ck, tk);                             \
    E4(SC, 4);                                    \
    E4(SC, 8);                                    \
    O = pv_lo(v0, SC, O, idx);                    \
    v0 = LV0(cv, tv);                             \
    E4(SC, 12);                                   \
    O = pv_hi(v1, SC, O, idx);                    \
    v1 = LV1(cv, tv);                             \
    SBAR();                                       \
  }
  for (int c = 0; c < 16; ++c) {
    const unsigned cur = (c & 3) * 16384, nxt = ((c + 1) & 3) * 16384;
    STEP(SA, SB, cur, 2, cur, 1);
    STEP(SB, SA, cur, 3, cur, 2);
    WAIT_BAR(2);
    RING_ISSUE(c + 3);
    STEP(SA, SB, nxt, 0, cur, 3);
    STEP(SB, SA, nxt, 1, nxt, 0);
  }
  WAIT_VM(0);
  float lsum = h ? O[8] : O[12];
  float oabs = 0.f;
#pragma unroll
  for (int i = 0; i < 12; ++i) oabs += fabsf(O[i]);
  const bool ok = (lsum >= 9.094947e-13f) && (oabs < 3.0e38f);
  if (__builtin_expect(__any(!ok), 0)) {
    const char* ga = kvg + l * 16;
    const char* gb = kvg + 3072 + l * 8;
    if (h == 1) qb = s16x4{0, 0, 0, 0};
    float m = -INFINITY;
    for (int t = 0; t < 64; ++t) {
      f32x16 s = MFMA32(*(const bf16x8*)(ga + t * 4096), qa, zero);
      s = MFMA8(*(const s16x4*)(gb + t * 4096), qb, s);
#pragma unroll
      for (int i = 0; i < 16; ++i) m = fmaxf(m, s[i]);
    }
    m = fmaxf(m, __shfl_xor(m, 32));
    const float m0 = rbf(m), m1 = rbf(m - m0), m2 = rbf(m - m0 - m1);
    if (h == 1) qb = __builtin_bit_cast(s16x4, u32x2{cvtpk(-m0, -m1), cvtpk(-m2, 0.f)});
    O = zero;
    for (int t = 0; t < 64; ++t) {
      f32x16 s = MFMA32(*(const bf16x8*)(ga + t * 4096), qa, zero);
      s = MFMA8(*(const s16x4*)(gb + t * 4096), qb, s);
#pragma unroll
      for (int i = 0; i < 16; ++i) s[i] = EX2(s[i]);
      O = pv_lo(*(const bf16x8*)(ga + t * 4096 + 1024), s, O, idx);
      O = pv_hi(*(const bf16x8*)(ga + t * 4096 + 2048), s, O, idx);
    }
    lsum = h ? O[8] : O[12];
  }
  const float inv = 1.0f / lsum;
  char* orow = ws + WS_O + (size_t)srow * 1280 + head * 40 + 8 * h;
  *(u32x2*)(orow) = u32x2{cvtpk(O[0] * inv, O[1] * inv), cvtpk(O[2] * inv, O[3] * inv)};
  *(u32x2*)(orow + 16) = u32x2{cvtpk(O[4] * inv, O[5] * inv), cvtpk(O[6] * inv, O[7] * inv)};
  if (h == 0) *(u32x2*)(orow + 32) = u32x2{cvtpk(O[8] * inv, O[9] * inv), cvtpk(O[10] * inv, O[11] * inv)};
}

__device__ __forceinline__ void conv_wave(const char* __restrict__ hk, const char* __restrict__ sd, char* ws,
                                          int bid, int j, int l, unsigned lds0) {
  const int nch = bid < 128 ? 3 : 2;
  const int ch0 = bid < 128 ? 3 * bid : 384 + 2 * (bid - 128);
  const unsigned cv0 = lds0 + M_CV;
  for (int c = 0; c < nch; ++c) {
    const char* hb = hk + (size_t)(ch0 + c) * 4096;
    const char* ub = ws + WS_UT + (size_t)(ch0 + c) * 8192;
#pragma unroll
    for (int i = 0; i < 4; ++i) {
      int e = 2 + 2 * j + 512 * i + 8 * l;
      e = e > 2040 ? 2040 : e;
      glds16(hb, e * 2, cv0 + c * CV_CH + j * CV_H + 64 + i * 1024);
    }
#pragma unroll
    for (int i = 0; i < 2; ++i)
      glds16(ub, (j >> 1) * 4096 + (j & 1) * 2048 + i * 1024 + l * 16,
             cv0 + c * CV_CH + 4 * CV_H + (j >> 1) * CV_U + (j & 1) * 2048 + i * 1024);
  }
  float dval[3];
  unsigned hv[3];
#pragma unroll
  for (int c = 0; c < 3; ++c) {
    const int cc = c < nch ? c : 0;
    const int e = 2 * (l & 15) - 30 + 2 * j;
    hv[c] = *(const unsigned*)(hk + (size_t)(ch0 + cc) * 4096 + (e < 0 ? 0 : e) * 2);
    dval[c] = __uint_as_float((unsigned)(*(const u16*)(sd + (ch0 + cc) * 2)) << 16);
  }
  BAR_ONLY();
  BAR_ONLY();
#pragma unroll
  for (int c = 0; c < 3; ++c) {
    const int e = 2 * (l & 15) - 30 + 2 * j;
    if (l < 16) *(LDS_AS unsigned*)(size_t)(cv0 + c * CV_CH + j * CV_H + l * 4) = e < 0 ? 0u : hv[c];
    if (j < 2 && l < 28) *(LDS_AS u32x4*)(size_t)(cv0 + c * CV_CH + 4 * CV_H + j * CV_U + 4096 + l * 16) = u32x4{0, 0, 0, 0};
  }
  WAIT_BAR(0);
  int bars = 14;
  const int i16 = l & 15, kq = l >> 4;
  const unsigned aa = cv0 + (i16 & 3) * CV_H + 16 * ((i16 >> 2) + kq);
  const unsigned ba = cv0 + 4 * CV_H + (i16 & 1) * CV_U + 2 * (2016 - 32 * (i16 >> 1) + 8 * kq);
  const int jj0 = (30 - 2 * (i16 & 3) - 8 * (i16 >> 2) - 8 * kq);
  int phase = 0;
  for (int c = 0; c < nch; ++c) {
    const unsigned db = __float_as_uint(c == 0 ? dval[0] : (c == 1 ? dval[1] : dval[2])) >> 16;
    const u32x4 dm = {jj0 == 0 ? db : 0u, jj0 == 2 ? db : 0u, jj0 == 4 ? db : 0u, jj0 == 6 ? db : 0u};
    const bf16x8 dfrag = __builtin_bit_cast(bf16x8, dm);
    for (int hi = 0; hi < 2; ++hi) {
      const int m = hi ? 7 - j : j;
      unsigned ap = aa + c * CV_CH, bp = ba + c * CV_CH - 512 * m;
      f32x4 acc = {0.f, 0.f, 0.f, 0.f};
      acc = MFMA16(dfrag, lds_b128((lds_ptr)(size_t)bp), acc);
      for (int gI = 0; gI <= m; ++gI) {
#pragma unroll
        for (int t = 0; t < 8; ++t)
          acc = MFMA16(lds_b128((lds_ptr)(size_t)(ap + 64 * t)), lds_b128((lds_ptr)(size_t)(bp + 64 * t)), acc);
        ap += 512;
        bp += 512;
        phase ^= 1;
        if (phase == 0 && bars > 0) {
          BAR_ONLY();
          --bars;
        }
      }
      *(u32x2*)(ws + WS_ZT + (size_t)(ch0 + c) * 4096 + (256 * m + 32 * (i16 >> 1) + 8 * kq + 4 * (i16 & 1)) * 2) =
          u32x2{cvtpk(acc[0], acc[1]), cvtpk(acc[2], acc[3])};
    }
  }
}

__global__ void __launch_bounds__(768, 3)
k_main(const char* __restrict__ hk, const char* __restrict__ sd, char* __restrict__ ws) {
  extern __shared__ __attribute__((aligned(16))) char smem[];
  const unsigned lds0 = (unsigned)(size_t)(lds_ptr)smem;
  const int bid = blockIdx.x;
  const int qt = (bid & 7) * 4 + ((bid >> 3) & 3), hg = bid >> 5;
  const int l = threadIdx.x & 63;
  const int w = __builtin_amdgcn_readfirstlane(threadIdx.x >> 6);
  if (w < 8) attn_wave(ws, qt, hg, w, l, lds0);
  else conv_wave(hk, sd, ws, bid, w - 8, l, lds0);
}

constexpr int O_Z = G_END;
constexpr int O_X = O_Z + 12288;
constexpr int O_G = O_X + 12288;
constexpr int O_BYTES = O_G + 4096;

template <int NH>
__device__ __forceinline__ void out_body(const char* x, const char* wo, char* ws, char* out, int qt, int hg, int w,
                                         int l, unsigned lds0, const GemmLane& g) {
  const int s0 = qt * 64;
  const char* abase = ws + WS_O + (size_t)s0 * 1280;
  const char* wbase = wo + hg * 160;
  gemm_issue(g, abase, wbase, 0, 0, w, lds0);
  gemm_issue(g, abase, wbase, 1, 1, w, lds0);
  gemm_issue(g, abase, wbase, 2, 2, w, lds0);
#pragma unroll
  for (int i = 0; i < 3; ++i) {
    int n = 192 * w + 64 * i + l;
    n = n < 640 ? n : 639;
    glds16(ws + WS_ZT, (hg * 80 + (n >> 3)) * 4096 + s0 * 2 + (n & 7) * 16, lds0 + O_Z + (3 * w + i) * 1024);
    const int rr = n / 10, cc = n - rr * 10;
    glds16(x, (s0 + rr) * 1280 + hg * 160 + cc * 16, lds0 + O_X + (3 * w + i) * 1024);
  }
  glds16(ws + WS_G, s0 * 4 + (l & 15) * 16, lds0 + O_G + w * 1024);
  f32x4 acc[2][3];
#pragma unroll
  for (int a = 0; a < 2; ++a)
#pragma unroll
    for (int t = 0; t < 3; ++t) acc[a][t] = f32x4{0.f, 0.f, 0.f, 0.f};
  WAIT_BAR(25);
  gemm_chunk<NH, 0>(g, acc, 0, 0, 0);
  WAIT_BAR(16);
  gemm_issue(g, abase, wbase, 3, 0, w, lds0);
  gemm_chunk<NH, 0>(g, acc, 1, 0, 0);
  WAIT_BAR(16);
  gemm_issue(g, abase, wbase, 4, 1, w, lds0);
  gemm_chunk<NH, 0>(g, acc, 2, 0, 0);
  WAIT_BAR(9);
  gemm_chunk<NH, 0>(g, acc, 0, 0, 0);
  WAIT_BAR(0);
  gemm_chunk<NH, 0>(g, acc, 1, 0, 0);
  const int sp = w & 1, kq = l >> 4;
  constexpr int NW = (NH == 0) ? 3 : 2;
#pragma unroll
  for (int a = 0; a < 2; ++a) {
    const int sl = 32 * sp + 16 * a + (l & 15);
    const float gt = *(const LDS_AS float*)(size_t)(lds0 + O_G + sl * 4);
    const float og = rbf(1.0f - gt);
    const int zp = (sl & ~7) + ((sl & 1) << 2) + ((sl & 7) >> 1);
#pragma unroll
    for (int t = 0; t < NW; ++t) {
      const int n = 48 * NH + 16 * t + 4 * kq;
      const u32x2 xv = *(const LDS_AS u32x2*)(size_t)(lds0 + O_X + sl * 160 + n * 2);
      float r[4];
#pragma unroll
      for (int e = 0; e < 4; ++e) {
        const float z = __uint_as_float((unsigned)(*(const LDS_AS u16*)(size_t)(lds0 + O_Z + (n + e) * 128 + zp * 2)) << 16);
        const float xq = (e & 1) ? bf_hi(xv[e >> 1]) : bf_lo(xv[e >> 1]);
        const float av = rbf(xq * z);
        const float bv = rbf(acc[a][t][e]);
        r[e] = rbf(gt * av) + rbf(og * bv);
      }
      *(u32x2*)(out + (size_t)(s0 + sl) * 1280 + (hg * 80 + n) * 2) = u32x2{cvtpk(r[0], r[1]), cvtpk(r[2], r[3])};
    }
  }
}

__global__ void __launch_bounds__(256, 1)
k_out(const char* __restrict__ x, const char* __restrict__ wo, char* __restrict__ ws, char* __restrict__ out) {
  extern __shared__ __attribute__((aligned(16))) char smem[];
  const unsigned lds0 = (unsigned)(size_t)(lds_ptr)smem;
  const int bid = blockIdx.x;
  const int qt = (bid & 7) * 4 + ((bid >> 3) & 3), hg = bid >> 5;
  const int l = threadIdx.x & 63;
  const int w = __builtin_amdgcn_readfirstlane(threadIdx.x >> 6);
  GemmLane g;
  gemm_setup(g, w, l, lds0);
  if (w < 2) out_body<0>(x, wo, ws, out, qt, hg, w, l, lds0, g);
  else out_body<1>(x, wo, ws, out, qt, hg, w, l, lds0, g);
}

extern "C" void kernel_launch(void* const* d_in, const int* in_sizes, int n_in, void* d_out, int out_size, void* d_ws,
                              size_t ws_size, hipStream_t stream) {
  const char* x = (const char*)d_in[0];
  const char* kin = (const char*)d_in[1];
  const char* vin = (const char*)d_in[2];
  const char* shk = (const char*)d_in[3];
  const char* hk = (const char*)d_in[4];
  const char* sd = (const char*)d_in[5];
  const char* wq = (const char*)d_in[7];
  const char* wk = (const char*)d_in[8];
  const char* wv = (const char*)d_in[9];
  const char* wo = (const char*)d_in[10];
  const char* gw = (const char*)d_in[11];
  char* ws = (char*)d_ws;
  (void)hipFuncSetAttribute((const void*)k_prep, hipFuncAttributeMaxDynamicSharedMemorySize, P_BYTES);
  (void)hipFuncSetAttribute((const void*)k_main, hipFuncAttributeMaxDynamicSharedMemorySize, M_BYTES);
  (void)hipFuncSetAttribute((const void*)k_out, hipFuncAttributeMaxDynamicSharedMemorySize, O_BYTES);
  k_prep<<<256, 256, P_BYTES, stream>>>(x, kin, vin, shk, wq, wk, wv, gw, ws);
  k_main<<<256, 768, M_BYTES, stream>>>(hk, sd, ws);
  k_out<<<256, 256, O_BYTES, stream>>>(x, wo, ws, (char*)d_out);
}
```

```cpp
#include <hip/hip_runtime.h>
#include <hip/hip_bf16.h>
#include <stdint.h>

typedef __attribute__((ext_vector_type(8))) __bf16 bf16x8;
typedef __attribute__((ext_vector_type(4))) __bf16 bf16x4;
typedef __attribute__((ext_vector_type(16))) __bf16 bf16x16;
typedef __attribute__((ext_vector_type(4))) short s16x4;
typedef __attribute__((ext_vector_type(16))) float f32x16;
typedef __attribute__((ext_vector_type(8))) float f32x8;
typedef __attribute__((ext_vector_type(4))) float f32x4;
typedef __attribute__((ext_vector_type(4))) unsigned u32x4;
typedef __attribute__((ext_vector_type(2))) unsigned u32x2;
typedef unsigned short u16;
#define LDS_AS __attribute__((address_space(3)))
typedef LDS_AS char* lds_ptr;

constexpr int S = 2048, D = 640;
constexpr float QSCALE = 0.22360679774997896f * 1.4426950408889634f;

constexpr size_t WS_QP = 0;
constexpr size_t WS_O = WS_QP + (size_t)S * D * 2;
constexpr size_t WS_ZT = WS_O + (size_t)S * D * 2;
constexpr size_t WS_UT = WS_ZT + (size_t)S * D * 2;
constexpr size_t WS_KV = WS_UT + (size_t)S * D * 4;
constexpr size_t WS_KN = WS_KV + 64 * 4096;
constexpr size_t WS_G = WS_KN + 1024;

#define STAMP_DECL
#define STAMP(i)
#define STAMP_FLUSH(k)
#define SBAR() __builtin_amdgcn_sched_barrier(0)
#define PIN(x) asm volatile("" : "+v"(x))
#define WAIT_VM(N) asm volatile("s_waitcnt vmcnt(" #N ")" ::: "memory")
#define WAIT_BAR(N) asm volatile("s_waitcnt vmcnt(" #N ") lgkmcnt(0)\n\ts_barrier" ::: "memory")
#define BAR_ONLY() asm volatile("s_waitcnt lgkmcnt(0)\n\ts_barrier" ::: "memory")

__device__ __forceinline__ unsigned cvtpk(float lo, float hi) {
  unsigned r;
  asm("v_cvt_pk_bf16_f32 %0, %1, %2" : "=v"(r) : "v"(lo), "v"(hi));
  return r;
}
__device__ __forceinline__ float bf_lo(unsigned w) { return __uint_as_float(w << 16); }
__device__ __forceinline__ float bf_hi(unsigned w) { return __uint_as_float(w & 0xffff0000u); }
__device__ __forceinline__ float rbf(float x) { return bf_lo(cvtpk(x, x)); }

__device__ __forceinline__ void glds16(const void* base, unsigned off, unsigned lds) {
  unsigned sv;
  asm volatile("s_mov_b32 %0, m0\n\ts_mov_b32 m0, %3\n\ts_nop 0\n\tglobal_load_lds_dwordx4 %1, %2\n\ts_mov_b32 m0, %0"
               : "=&s"(sv) : "v"(off), "s"(base), "s"(lds) : "memory");
}
__device__ __forceinline__ void gstore16(void* base, unsigned off, u32x4 v) {
  asm volatile("global_store_dwordx4 %0, %1, %2\n\ts_nop 1" :: "v"(off), "v"(v), "s"(base) : "memory");
}
__device__ __forceinline__ bf16x8 lds_b128(lds_ptr p) { return *(const LDS_AS bf16x8*)p; }
__device__ __forceinline__ s16x4 lds_tr(lds_ptr p) {
  return __builtin_amdgcn_ds_read_tr16_b64_v4i16((LDS_AS s16x4*)p);
}
__device__ __forceinline__ bf16x8 cat4(s16x4 a, s16x4 b) {
  return __builtin_bit_cast(bf16x8, __builtin_shufflevector(a, b, 0, 1, 2, 3, 4, 5, 6, 7));
}
#define MFMA16(a, b, c) __builtin_amdgcn_mfma_f32_16x16x32_bf16(a, b, c, 0, 0, 0)

constexpr int G_XB = 0;
constexpr int G_WB = G_XB + 3 * 16384;
constexpr int G_END = G_WB + 3 * 20480;

struct GemmLane {
  unsigned xoff[4], woff[5];
  unsigned xa[4];
  unsigned wa0, wa1;
};

__device__ __forceinline__ void gemm_setup(GemmLane& g, int w, int l, unsigned lds0) {
#pragma unroll
  for (int i = 0; i < 4; ++i) {
    const int r = 4 * i + (l >> 4);
    g.xoff[i] = (16 * w + r) * 1280 + 16 * ((l & 15) ^ r);
  }
#pragma unroll
  for (int i = 0; i < 5; ++i) {
    const int n = 320 * w + 64 * i + l;
    const int pr = n / 10, cc = n - pr * 10;
    const int k = pr ^ (((pr >> 3) & 1) << 2);
    g.woff[i] = k * 1280 + 16 * cc;
  }
  const int sp = w & 1, nh = w >> 1;
  const int r = l & 15, kq = l >> 4;
#pragma unroll
  for (int ks = 0; ks < 4; ++ks) g.xa[ks] = lds0 + G_XB + (32 * sp + r) * 256 + 16 * ((4 * ks + kq) ^ r);
  const int q = (l & 15) >> 2, p = l & 3;
  g.wa0 = lds0 + G_WB + (8 * kq + 4 * (kq & 1) + q) * 160 + 8 * p + 96 * nh;
  g.wa1 = lds0 + G_WB + (8 * kq + 4 * (1 - (kq & 1)) + q) * 160 + 8 * p + 96 * nh;
}
__device__ __forceinline__ void gemm_issue(const GemmLane& g, const char* abase, const char* wbase, int kc, int buf,
                                           int w, unsigned lds0) {
  const char* ab = abase + kc * 256;
  const char* wb = wbase + (size_t)kc * 128 * 1280;
#pragma unroll
  for (int i = 0; i < 4; ++i) glds16(ab, g.xoff[i], lds0 + G_XB + buf * 16384 + (4 * w + i) * 1024);
#pragma unroll
  for (int i = 0; i < 5; ++i) glds16(wb, g.woff[i], lds0 + G_WB + buf * 20480 + (5 * w + i) * 1024);
}
template <int NH, int EXMODE>
__device__ __forceinline__ void gemm_chunk(const GemmLane& g, f32x4 (&acc)[2][3], int buf, unsigned exa0,
                                           unsigned exa1) {
#pragma unroll
  for (int ks = 0; ks < 4; ++ks) {
    bf16x8 xf[2], wf[3];
#pragma unroll
    for (int a = 0; a < 2; ++a) xf[a] = lds_b128((lds_ptr)(size_t)(g.xa[ks] + buf * 16384 + a * 4096));
    constexpr int NW = (NH == 0) ? 3 : 2;
#pragma unroll
    for (int t = 0; t < NW; ++t)
      wf[t] = cat4(lds_tr((lds_ptr)(size_t)(g.wa0 + buf * 20480 + ks * 5120 + t * 32)),
                   lds_tr((lds_ptr)(size_t)(g.wa1 + buf * 20480 + ks * 5120 + t * 32)));
    if (NH == 1 && EXMODE != 0)
      wf[2] = cat4(lds_tr((lds_ptr)(size_t)(exa0 + ks * 1024)), lds_tr((lds_ptr)(size_t)(exa1 + ks * 1024)));
#pragma unroll
    for (int a = 0; a < 2; ++a) {
#pragma unroll
      for (int t = 0; t < NW; ++t) acc[a][t] = MFMA16(wf[t], xf[a], acc[a][t]);
      if (NH == 1 && EXMODE == 1) acc[a][2] = MFMA16(wf[2], xf[a], acc[a][2]);
      if (NH == 1 && EXMODE == 2) acc[a][2] = MFMA16(xf[a], wf[2], acc[a][2]);
    }
  }
}

constexpr int P_EX = G_END;
constexpr int P_KV = P_EX + 20480;
constexpr int P_GW = P_KV + 4 * 6144;
constexpr int P_SK = P_GW + 2048;
constexpr int P_BYTES = P_SK + 1024;

template <int NH, int EXMODE>
__device__ __forceinline__ void prep_body(const char* x, const char* kin, const char* vin, const char* wq, char* ws,
                                          int qt, int hg, int w, int l, unsigned lds0, const GemmLane& g) {
  STAMP_DECL; STAMP(0);
  const int s0 = qt * 64;
  const char* abase = x + (size_t)s0 * 1280;
  const char* wbase = wq + hg * 160;
  gemm_issue(g, abase, wbase, 0, 0, w, lds0);
  gemm_issue(g, abase, wbase, 1, 1, w, lds0);
  gemm_issue(g, abase, wbase, 2, 2, w, lds0);
  {
    const int sw = s0 + 16 * w;
#pragma unroll
    for (int i = 0; i < 6; ++i) {
      int n = 64 * i + l;
      n = n < 370 ? n : 369;
      const bool isk = n < 200;
      const int m = isk ? n : n - 200;
      const int rr = m / 10, cc = m - rr * 10;
      int row = sw + rr - (isk ? 4 : 1);
      row = row < 0 ? 0 : row;
      const unsigned off = row * 1280 + hg * 160 + cc * 16;
      if (i < 3) glds16(kin, off, lds0 + P_KV + w * 6144 + i * 1024);
      else if (i > 3) glds16(vin, off, lds0 + P_KV + w * 6144 + i * 1024);
      else {
        const char* b = kin;
        const long long dv = (long long)(vin - kin);
        (void)b; (void)dv;
        if (isk) glds16(kin, off, lds0 + P_KV + w * 6144 + i * 1024);
        if (!isk) glds16(vin, off, lds0 + P_KV + w * 6144 + i * 1024);
      }
    }
  }
  f32x4 acc[2][3];
#pragma unroll
  for (int a = 0; a < 2; ++a)
#pragma unroll
    for (int t = 0; t < 3; ++t) acc[a][t] = f32x4{0.f, 0.f, 0.f, 0.f};
  const int kq = l >> 4, q4 = (l & 15) >> 2, p4 = l & 3;
  const unsigned exa0 = lds0 + P_EX + (8 * kq + 4 * (kq & 1) + q4) * 32 + 8 * p4;
  const unsigned exa1 = lds0 + P_EX + (8 * kq + 4 * (1 - (kq & 1)) + q4) * 32 + 8 * p4;
  const int grow = 8 * hg + 2 * w + (l >> 5), gi = l & 31;
  const unsigned gxa = lds0 + G_XB + grow * 256 + 16 * ((gi >> 1) ^ (grow & 15)) + 8 * (gi & 1);
  const unsigned gwa = lds0 + P_GW + gi * 8;
  float gacc = 0.f;
#define GATE(kc, buf)                                                                   \
  {                                                                                     \
    const u32x2 xv = *(const LDS_AS u32x2*)(size_t)(gxa + (buf) * 16384);               \
    const u32x2 wv = *(const LDS_AS u32x2*)(size_t)(gwa + (kc) * 256);                  \
    const unsigned p0 = cvtpk(bf_lo(xv[0]) * bf_lo(wv[0]), bf_hi(xv[0]) * bf_hi(wv[0])); \
    const unsigned p1 = cvtpk(bf_lo(xv[1]) * bf_lo(wv[1]), bf_hi(xv[1]) * bf_hi(wv[1])); \
    gacc += (bf_lo(p0) + bf_hi(p0)) + (bf_lo(p1) + bf_hi(p1));                          \
  }
  STAMP(1);
  WAIT_BAR(25);
  STAMP(2);
  gemm_chunk<NH, EXMODE>(g, acc, 0, exa0, exa1);
  GATE(0, 0);
  WAIT_BAR(16);
  gemm_issue(g, abase, wbase, 3, 0, w, lds0);
  gemm_chunk<NH, EXMODE>(g, acc, 1, exa0 + 4096, exa1 + 4096);
  GATE(1, 1);
  WAIT_BAR(16);
  gemm_issue(g, abase, wbase, 4, 1, w, lds0);
  STAMP(3);
  WAIT_VM(18);
  {
    const int dp = l < 40 ? l : 39;
    const unsigned ka = lds0 + P_KV + w * 6144 + dp * 4;
    const u32x4 sk = *(const LDS_AS u32x4*)(size_t)(lds0 + P_SK + dp * 16);
    const float a0 = bf_lo(sk[0]), a1 = bf_hi(sk[0]), a2 = bf_lo(sk[1]), a3 = bf_hi(sk[1]);
    const float b0 = bf_lo(sk[2]), b1 = bf_hi(sk[2]), b2 = bf_lo(sk[3]), b3 = bf_hi(sk[3]);
    char* ut = ws + WS_UT;
#pragma unroll
    for (int oc = 0; oc < 2; ++oc) {
      const int so = s0 + 16 * w + 8 * oc;
      float kl[12], kh[12];
#pragma unroll
      for (int j = 0; j < 12; ++j) {
        unsigned kk = *(const LDS_AS unsigned*)(size_t)(ka + (8 * oc + j) * 160);
        if (j < 4) kk = (so == 0) ? 0u : kk;
        kl[j] = bf_lo(kk);
        kh[j] = bf_hi(kk);
      }
      float ul[9], uh[9];
#pragma unroll
      for (int j = 0; j < 9; ++j) {
        unsigned vv = *(const LDS_AS unsigned*)(size_t)(ka + 3200 + (8 * oc + j) * 160);
        if (j == 0) vv = (so == 0) ? 0u : vv;
        const float sl = fmaf(kl[j + 3], a0, fmaf(kl[j + 2], a1, fmaf(kl[j + 1], a2, kl[j] * a3)));
        const float sh = fmaf(kh[j + 3], b0, fmaf(kh[j + 2], b1, fmaf(kh[j + 1], b2, kh[j] * b3)));
        ul[j] = sl * bf_lo(vv);
        uh[j] = sh * bf_hi(vv);
      }
      const unsigned pos = (2040 - so) * 2;
      const unsigned ch = (hg * 80 + 2 * dp) * 8192;
      const u32x4 l1 = {cvtpk(ul[8], ul[7]), cvtpk(ul[6], ul[5]), cvtpk(ul[4], ul[3]), cvtpk(ul[2], ul[1])};
      const u32x4 l0 = {cvtpk(ul[7], ul[6]), cvtpk(ul[5], ul[4]), cvtpk(ul[3], ul[2]), cvtpk(ul[1], ul[0])};
      const u32x4 h1 = {cvtpk(uh[8], uh[7]), cvtpk(uh[6], uh[5]), cvtpk(uh[4], uh[3]), cvtpk(uh[2], uh[1])};
      const u32x4 h0 = {cvtpk(uh[7], uh[6]), cvtpk(uh[5], uh[4]), cvtpk(uh[3], uh[2]), cvtpk(uh[1], uh[0])};
      if (l < 40) {
        gstore16(ut, ch + pos, l0);
        gstore16(ut, ch + 4096 + pos, l1);
        gstore16(ut, ch + 8192 + pos, h0);
        gstore16(ut, ch + 8192 + 4096 + pos, h1);
      }
    }
  }
  STAMP(4);
  gemm_chunk<NH, EXMODE>(g, acc, 2, exa0 + 8192, exa1 + 8192);
  GATE(2, 2);
  WAIT_BAR(17);
  gemm_chunk<NH, EXMODE>(g, acc, 0, exa0 + 12288, exa1 + 12288);
  GATE(3, 0);
  WAIT_BAR(8);
  gemm_chunk<NH, EXMODE>(g, acc, 1, exa0 + 16384, exa1 + 16384);
  GATE(4, 1);
#undef GATE
  STAMP(5);
  const int sp = w & 1;
  {
    constexpr int NW = (NH == 0) ? 3 : 2;
#pragma unroll
    for (int a = 0; a < 2; ++a)
#pragma unroll
      for (int t = 0; t < NW; ++t) {
        const int s = s0 + 32 * sp + 16 * a + (l & 15);
        const int n = hg * 80 + 48 * NH + 16 * t + 4 * kq;
        const u32x2 v = {cvtpk(acc[a][t][0] * QSCALE, acc[a][t][1] * QSCALE),
                         cvtpk(acc[a][t][2] * QSCALE, acc[a][t][3] * QSCALE)};
        *(u32x2*)(ws + WS_QP + (size_t)s * 1280 + n * 2) = v;
      }
  }
  {
    float t = gacc;
#pragma unroll
    for (int o = 1; o < 32; o <<= 1) t += __shfl_xor(t, o);
    const float z = rbf(t);
    const float sg = rbf(1.0f / (1.0f + __expf(-z)));
    if (gi == 0) ((float*)(ws + WS_G))[s0 + grow] = sg;
  }
  char* kvt = ws + WS_KV + (size_t)(2 * qt + sp) * 4096;
  if (NH == 1 && EXMODE != 0) {
#pragma unroll
    for (int a = 0; a < 2; ++a) {
      const u32x2 v = {cvtpk(acc[a][2][0], acc[a][2][1]), cvtpk(acc[a][2][2], acc[a][2][3])};
      if (EXMODE == 2) {
        *(u32x2*)(kvt + 1024 + 1024 * a + 16 * ((l & 15) + 32 * (kq >> 1)) + 8 * (kq & 1)) = v;
      } else {
        const int key = 16 * a + (l & 15);
        float nn = bf_lo(v[0]) * bf_lo(v[0]) + bf_hi(v[0]) * bf_hi(v[0]) + bf_lo(v[1]) * bf_lo(v[1]) +
                   bf_hi(v[1]) * bf_hi(v[1]);
        if (hg == 0) {
          *(u32x2*)(kvt + 16 * (key + 32 * (kq >> 1)) + 8 * (kq & 1)) = v;
          nn += __shfl_xor(nn, 16);
          nn += __shfl_xor(nn, 32);
        } else {
          if (kq == 1) *(u32x2*)(kvt + 3072 + 8 * key) = v;
          if (kq == 3) {
            char* vb = kvt + 1024 + 1024 * a + 16 * (16 + 32 * ((key >> 3) & 1)) + 2 * (key & 7);
            *(u16*)(vb) = (u16)(v[0] & 0xffff);
            *(u16*)(vb + 16) = (u16)(v[0] >> 16);
            *(u16*)(vb + 32) = (u16)(v[1] & 0xffff);
            *(u16*)(vb + 48) = (u16)(v[1] >> 16);
          }
          nn = (kq == 1) ? nn : 0.f;
          nn = fmaxf(nn, __shfl_xor(nn, 16));
        }
#pragma unroll
        for (int o = 1; o < 16; o <<= 1) nn = fmaxf(nn, __shfl_xor(nn, o));
        if (l == 0) ((float*)(ws + WS_KN))[(hg == 0 ? 0 : 128) + qt * 4 + 2 * sp + a] = nn;
      }
    }
  }
  if (hg == 3 && NH == 0) {
    if (l < 32) *(u32x2*)(kvt + 3072 + 256 + 8 * l) = u32x2{0x3f803f80u, 0x00003f80u};
    if (l < 48) {
      const int d = 20 + (l % 12), q = l / 12;
      const unsigned c = (d == 20 || d == 24) ? 0x3f803f80u : 0u;
      *(u32x4*)(kvt + 1024 + 1024 * (q >> 1) + 16 * (d + 32 * (q & 1))) = u32x4{c, c, c, c};
    }
  }
  STAMP(6); WAIT_VM(0); STAMP(7);
  STAMP_FLUSH(0);
}

__global__ void __launch_bounds__(256, 1)
k_prep(const char* __restrict__ x, const char* __restrict__ kin, const char* __restrict__ vin,
       const char* __restrict__ shk, const char* __restrict__ wq, const char* __restrict__ wk,
       const char* __restrict__ wv, const char* __restrict__ gw, char* __restrict__ ws) {
  extern __shared__ __attribute__((aligned(16))) char smem[];
  const unsigned lds0 = (unsigned)(size_t)(lds_ptr)smem;
  const int bid = blockIdx.x;
  const int qt = (bid & 7) * 4 + ((bid >> 3) & 3), hg = bid >> 5;
  const int l = threadIdx.x & 63;
  const int w = __builtin_amdgcn_readfirstlane(threadIdx.x >> 6);
  if (hg < 3) {
#pragma unroll
    for (int i = 0; i < 5; ++i) {
      const int n = 320 * w + 64 * i + l;
      const int pr = n >> 1, cc = n & 1;
      const int k = pr ^ (((pr >> 3) & 1) << 2);
      const unsigned dst = lds0 + P_EX + (5 * w + i) * 1024;
      if (hg == 0) glds16(wk, k * 40 + 16 * cc, dst);
      else if (hg == 1) glds16(wv, k * 40 + 16 * cc, dst);
      else {
        if (cc == 0) glds16(wk, k * 40 + 24, dst);
        if (cc == 1) glds16(wv, k * 40 + 24, dst);
      }
    }
  }
  if (w == 0) glds16(gw, l * 16, lds0 + P_GW);
  if (w == 1 && l < 16) glds16(gw, 1024 + l * 16, lds0 + P_GW + 1024);
  if (w == 2 && l < 40) glds16(shk, hg * 640 + l * 16, lds0 + P_SK);
  GemmLane g;
  gemm_setup(g, w, l, lds0);
  if (w < 2) prep_body<0, 0>(x, kin, vin, wq, ws, qt, hg, w, l, lds0, g);
  else if (hg == 1) prep_body<1, 2>(x, kin, vin, wq, ws, qt, hg, w, l, lds0, g);
  else if (hg < 3) prep_body<1, 1>(x, kin, vin, wq, ws, qt, hg, w, l, lds0, g);
  else prep_body<1, 0>(x, kin, vin, wq, ws, qt, hg, w, l, lds0, g);
}

constexpr int M_RING = 0;
constexpr int CV_H = 4160, CV_U = 4640, CV_CH = 4 * CV_H + 2 * CV_U;
constexpr int M_CV = 65536;
constexpr int M_BYTES = M_CV + 3 * CV_CH;

#define MFMA32(a, b, c) __builtin_amdgcn_mfma_f32_32x32x16_bf16(a, b, c, 0, 0, 0)
#define MFMA8(a, b, c) __builtin_amdgcn_mfma_f32_32x32x8bf16_1k(a, b, c, 0, 0, 0)
__device__ __forceinline__ f32x16 pv_lo(bf16x8 v, f32x16 p, f32x16 o, int idx) {
  const f32x8 h = __builtin_shufflevector(p, p, 0, 1, 2, 3, 4, 5, 6, 7);
  return __builtin_amdgcn_smfmac_f32_32x32x32_bf16(v, __builtin_bit_cast(bf16x16, h), o, idx, 0, 0);
}
__device__ __forceinline__ f32x16 pv_hi(bf16x8 v, f32x16 p, f32x16 o, int idx) {
  const f32x8 h = __builtin_shufflevector(p, p, 8, 9, 10, 11, 12, 13, 14, 15);
  return __builtin_amdgcn_smfmac_f32_32x32x32_bf16(v, __builtin_bit_cast(bf16x16, h), o, idx, 0, 0);
}
#define EX2(v) __builtin_amdgcn_exp2f(v)

__device__ __forceinline__ void attn_wave(char* ws, int qt, int hg, int w, int l, unsigned lds0) {
  STAMP_DECL; STAMP(0);
  const char* kvg = ws + WS_KV;
  const int h = l >> 5;
  const int head = hg * 4 + (w & 3);
  const int srow = qt * 64 + 32 * (w >> 2) + (l & 31);
  const unsigned dsrc = w * 2048 + l * 16;
  const unsigned ddst = lds0 + M_RING + w * 2048;
#define RING_ISSUE(c)                                                            \
  {                                                                              \
    const char* b_ = kvg + ((c) & 15) * 16384;                                   \
    glds16(b_, dsrc, ddst + ((c) & 3) * 16384);                                  \
    glds16(b_, dsrc + 1024, ddst + ((c) & 3) * 16384 + 1024);                    \
  }
  const char* qrow = ws + WS_QP + (size_t)srow * 1280 + head * 40;
  u32x4 q16 = *(const u32x4*)(qrow + 16 * h);
  u32x2 q8 = *(const u32x2*)(qrow + 32);
  const f32x4 kn4 = *(const f32x4*)(ws + WS_KN + (h * 128 + (l & 31) * 4) * 4);
  RING_ISSUE(0);
  RING_ISSUE(1);
  RING_ISSUE(2);
  float kn = fmaxf(fmaxf(kn4[0], kn4[1]), fmaxf(kn4[2], kn4[3]));
#pragma unroll
  for (int o = 1; o < 32; o <<= 1) kn = fmaxf(kn, __shfl_xor(kn, o));
  kn += __shfl_xor(kn, 32);
  float qn = 0.f;
#pragma unroll
  for (int i = 0; i < 4; ++i) qn += bf_lo(q16[i]) * bf_lo(q16[i]) + bf_hi(q16[i]) * bf_hi(q16[i]);
  if (h == 0) {
#pragma unroll
    for (int i = 0; i < 2; ++i) qn += bf_lo(q8[i]) * bf_lo(q8[i]) + bf_hi(q8[i]) * bf_hi(q8[i]);
  }
  qn += __shfl_xor(qn, 32);
  {
    const float mref = sqrtf(qn * kn) * 1.01f - 64.0f;
    const unsigned nb = cvtpk(-mref, 0.f);
    if (h == 1) q8 = u32x2{nb, 0u};
  }
  const bf16x8 qa = __builtin_bit_cast(bf16x8, q16);
  s16x4 qb = __builtin_bit_cast(s16x4, q8);
  const int idx = 0xdddd;
  const unsigned ra = lds0 + M_RING + l * 16, rb = lds0 + M_RING + 3072 + l * 8;
#define LK16(c, t) lds_b128((lds_ptr)(size_t)(ra + (c) + (t) * 4096))
#define LK8(c, t) (*(const LDS_AS s16x4*)(size_t)(rb + (c) + (t) * 4096))
#define LV0(c, t) lds_b128((lds_ptr)(size_t)(ra + (c) + (t) * 4096 + 1024))
#define LV1(c, t) lds_b128((lds_ptr)(size_t)(ra + (c) + (t) * 4096 + 2048))
  const f32x16 zero = {0.f, 0.f, 0.f, 0.f, 0.f, 0.f, 0.f, 0.f, 0.f, 0.f, 0.f, 0.f, 0.f, 0.f, 0.f, 0.f};
  f32x16 O = zero, SA, SB;
  STAMP(1);
  WAIT_BAR(4);
  STAMP(2);
  bf16x8 k16, v0, v1;
  s16x4 k8;
  k16 = LK16(0, 0);
  k8 = LK8(0, 0);
  SA = MFMA32(k16, qa, zero);
  SA = MFMA8(k8, qb, SA);
  k16 = LK16(0, 1);
  k8 = LK8(0, 1);
  v0 = LV0(0, 0);
  v1 = LV1(0, 0);
#define E4(X, i)                                                                           \
  X[i] = EX2(X[i]); X[i + 1] = EX2(X[i + 1]); X[i + 2] = EX2(X[i + 2]); X[i + 3] = EX2(X[i + 3]); \
  PIN(X); SBAR();
#define STEP(SC, SN, ck, tk, cv, tv)              \
  {                                               \
    SN = MFMA32(k16, qa, zero);                   \
    k16 = LK16(ck, tk);                           \
    E4(SC, 0);                                    \
    SN = MFMA8(k8, qb, SN);                       \
    k8 = LK8(ck, tk);                             \
    E4(SC, 4);                                    \
    E4(SC, 8);                                    \
    O = pv_lo(v0, SC, O, idx);                    \
    v0 = LV0(cv, tv);                             \
    E4(SC, 12);                                   \
    O = pv_hi(v1, SC, O, idx);                    \
    v1 = LV1(cv, tv);                             \
    SBAR();                                       \
  }
  for (int c = 0; c < 16; ++c) {
    const unsigned cur = (c & 3) * 16384, nxt = ((c + 1) & 3) * 16384;
    STEP(SA, SB, cur, 2, cur, 1);
    STEP(SB, SA, cur, 3, cur, 2);
    WAIT_BAR(2);
    RING_ISSUE(c + 3);
    STEP(SA, SB, nxt, 0, cur, 3);
    STEP(SB, SA, nxt, 1, nxt, 0);
  }
  STAMP(3);
  WAIT_VM(0);
  float lsum = h ? O[8] : O[12];
  float oabs = 0.f;
#pragma unroll
  for (int i = 0; i < 12; ++i) oabs += fabsf(O[i]);
  const bool ok = (lsum >= 9.094947e-13f) && (oabs < 3.0e38f);
  if (__builtin_expect(__any(!ok), 0)) {
    const char* ga = kvg + l * 16;
    const char* gb = kvg + 3072 + l * 8;
    if (h == 1) qb = s16x4{0, 0, 0, 0};
    float m = -INFINITY;
    for (int t = 0; t < 64; ++t) {
      f32x16 s = MFMA32(*(const bf16x8*)(ga + t * 4096), qa, zero);
      s = MFMA8(*(const s16x4*)(gb + t * 4096), qb, s);
#pragma unroll
      for (int i = 0; i < 16; ++i) m = fmaxf(m, s[i]);
    }
    m = fmaxf(m, __shfl_xor(m, 32));
    const float m0 = rbf(m), m1 = rbf(m - m0), m2 = rbf(m - m0 - m1);
    if (h == 1) qb = __builtin_bit_cast(s16x4, u32x2{cvtpk(-m0, -m1), cvtpk(-m2, 0.f)});
    O = zero;
    for (int t = 0; t < 64; ++t) {
      f32x16 s = MFMA32(*(const bf16x8*)(ga + t * 4096), qa, zero);
      s = MFMA8(*(const s16x4*)(gb + t * 4096), qb, s);
#pragma unroll
      for (int i = 0; i < 16; ++i) s[i] = EX2(s[i]);
      O = pv_lo(*(const bf16x8*)(ga + t * 4096 + 1024), s, O, idx);
      O = pv_hi(*(const bf16x8*)(ga + t * 4096 + 2048), s, O, idx);
    }
    lsum = h ? O[8] : O[12];
  }
  const float inv = 1.0f / lsum;
  char* orow = ws + WS_O + (size_t)srow * 1280 + head * 40 + 8 * h;
  *(u32x2*)(orow) = u32x2{cvtpk(O[0] * inv, O[1] * inv), cvtpk(O[2] * inv, O[3] * inv)};
  *(u32x2*)(orow + 16) = u32x2{cvtpk(O[4] * inv, O[5] * inv), cvtpk(O[6] * inv, O[7] * inv)};
  if (h == 0) *(u32x2*)(orow + 32) = u32x2{cvtpk(O[8] * inv, O[9] * inv), cvtpk(O[10] * inv, O[11] * inv)};
  STAMP(4); WAIT_VM(0); STAMP(5);
  STAMP_FLUSH(1);
}

__device__ __forceinline__ void conv_wave(const char* __restrict__ hk, const char* __restrict__ sd, char* ws,
                                          int bid, int j, int l, unsigned lds0) {
  STAMP_DECL; STAMP(0);
  const int nch = bid < 128 ? 3 : 2;
  const int ch0 = bid < 128 ? 3 * bid : 384 + 2 * (bid - 128);
  const unsigned cv0 = lds0 + M_CV;
  for (int c = 0; c < nch; ++c) {
    const char* hb = hk + (size_t)(ch0 + c) * 4096;
    const char* ub = ws + WS_UT + (size_t)(ch0 + c) * 8192;
#pragma unroll
    for (int i = 0; i < 4; ++i) {
      int e = 2 + 2 * j + 512 * i + 8 * l;
      e = e > 2040 ? 2040 : e;
      glds16(hb, e * 2, cv0 + c * CV_CH + j * CV_H + 64 + i * 1024);
    }
#pragma unroll
    for (int i = 0; i < 2; ++i)
      glds16(ub, (j >> 1) * 4096 + (j & 1) * 2048 + i * 1024 + l * 16,
             cv0 + c * CV_CH + 4 * CV_H + (j >> 1) * CV_U + (j & 1) * 2048 + i * 1024);
  }
  float dval[3];
  unsigned hv[3];
#pragma unroll
  for (int c = 0; c < 3; ++c) {
    const int cc = c < nch ? c : 0;
    const int e = 2 * (l & 15) - 30 + 2 * j;
    hv[c] = *(const unsigned*)(hk + (size_t)(ch0 + cc) * 4096 + (e < 0 ? 0 : e) * 2);
    dval[c] = __uint_as_float((unsigned)(*(const u16*)(sd + (ch0 + cc) * 2)) << 16);
  }
  BAR_ONLY();
  BAR_ONLY();
#pragma unroll
  for (int c = 0; c < 3; ++c) {
    const int e = 2 * (l & 15) - 30 + 2 * j;
    if (l < 16) *(LDS_AS unsigned*)(size_t)(cv0 + c * CV_CH + j * CV_H + l * 4) = e < 0 ? 0u : hv[c];
    if (j < 2 && l < 28) *(LDS_AS u32x4*)(size_t)(cv0 + c * CV_CH + 4 * CV_H + j * CV_U + 4096 + l * 16) = u32x4{0, 0, 0, 0};
  }
  STAMP(1);
  WAIT_BAR(0);
  STAMP(2);
  int bars = 14;
  const int i16 = l & 15, kq = l >> 4;
  const unsigned aa = cv0 + (i16 & 3) * CV_H + 16 * ((i16 >> 2) + kq);
  const unsigned ba = cv0 + 4 * CV_H + (i16 & 1) * CV_U + 2 * (2016 - 32 * (i16 >> 1) + 8 * kq);
  const int jj0 = (30 - 2 * (i16 & 3) - 8 * (i16 >> 2) - 8 * kq);
  int phase = 0;
  for (int c = 0; c < nch; ++c) {
    const unsigned db = __float_as_uint(c == 0 ? dval[0] : (c == 1 ? dval[1] : dval[2])) >> 16;
    const u32x4 dm = {jj0 == 0 ? db : 0u, jj0 == 2 ? db : 0u, jj0 == 4 ? db : 0u, jj0 == 6 ? db : 0u};
    const bf16x8 dfrag = __builtin_bit_cast(bf16x8, dm);
    for (int hi = 0; hi < 2; ++hi) {
      const int m = hi ? 7 - j : j;
      unsigned ap = aa + c * CV_CH, bp = ba + c * CV_CH - 512 * m;
      f32x4 acc = {0.f, 0.f, 0.f, 0.f};
      acc = MFMA16(dfrag, lds_b128((lds_ptr)(size_t)bp), acc);
      for (int gI = 0; gI <= m; ++gI) {
#pragma unroll
        for (int t = 0; t < 8; ++t)
          acc = MFMA16(lds_b128((lds_ptr)(size_t)(ap + 64 * t)), lds_b128((lds_ptr)(size_t)(bp + 64 * t)), acc);
        ap += 512;
        bp += 512;
        phase ^= 1;
        if (phase == 0 && bars > 0) {
          BAR_ONLY();
          --bars;
        }
      }
      *(u32x2*)(ws + WS_ZT + (size_t)(ch0 + c) * 4096 + (256 * m + 32 * (i16 >> 1) + 8 * kq + 4 * (i16 & 1)) * 2) =
          u32x2{cvtpk(acc[0], acc[1]), cvtpk(acc[2], acc[3])};
    }
  }
  STAMP(3); STAMP(4); WAIT_VM(0); STAMP(5);
  STAMP_FLUSH(1);
}

__global__ void __launch_bounds__(768, 3)
k_main(const char* __restrict__ hk, const char* __restrict__ sd, char* __restrict__ ws) {
  extern __shared__ __attribute__((aligned(16))) char smem[];
  const unsigned lds0 = (unsigned)(size_t)(lds_ptr)smem;
  const int bid = blockIdx.x;
  const int qt = (bid & 7) * 4 + ((bid >> 3) & 3), hg = bid >> 5;
  const int l = threadIdx.x & 63;
  const int w = __builtin_amdgcn_readfirstlane(threadIdx.x >> 6);
  if (w < 8) attn_wave(ws, qt, hg, w, l, lds0);
  else conv_wave(hk, sd, ws, bid, w - 8, l, lds0);
}

constexpr int O_Z = G_END;
constexpr int O_X = O_Z + 12288;
constexpr int O_G = O_X + 12288;
constexpr int O_BYTES = O_G + 4096;

template <int NH>
__device__ __forceinline__ void out_body(const char* x, const char* wo, char* ws, char* out, int qt, int hg, int w,
                                         int l, unsigned lds0, const GemmLane& g) {
  STAMP_DECL; STAMP(0);
  const int s0 = qt * 64;
  const char* abase = ws + WS_O + (size_t)s0 * 1280;
  const char* wbase = wo + hg * 160;
  gemm_issue(g, abase, wbase, 0, 0, w, lds0);
  gemm_issue(g, abase, wbase, 1, 1, w, lds0);
  gemm_issue(g, abase, wbase, 2, 2, w, lds0);
#pragma unroll
  for (int i = 0; i < 3; ++i) {
    int n = 192 * w + 64 * i + l;
    n = n < 640 ? n : 639;
    glds16(ws + WS_ZT, (hg * 80 + (n >> 3)) * 4096 + s0 * 2 + (n & 7) * 16, lds0 + O_Z + (3 * w + i) * 1024);
    const int rr = n / 10, cc = n - rr * 10;
    glds16(x, (s0 + rr) * 1280 + hg * 160 + cc * 16, lds0 + O_X + (3 * w + i) * 1024);
  }
  glds16(ws + WS_G, s0 * 4 + (l & 15) * 16, lds0 + O_G + w * 1024);
  f32x4 acc[2][3];
#pragma unroll
  for (int a = 0; a < 2; ++a)
#pragma unroll
    for (int t = 0; t < 3; ++t) acc[a][t] = f32x4{0.f, 0.f, 0.f, 0.f};
  STAMP(1);
  WAIT_BAR(25);
  STAMP(2);
  gemm_chunk<NH, 0>(g, acc, 0, 0, 0);
  WAIT_BAR(16);
  gemm_issue(g, abase, wbase, 3, 0, w, lds0);
  gemm_chunk<NH, 0>(g, acc, 1, 0, 0);
  WAIT_BAR(16);
  gemm_issue(g, abase, wbase, 4, 1, w, lds0);
  gemm_chunk<NH, 0>(g, acc, 2, 0, 0);
  STAMP(3);
  WAIT_BAR(9);
  gemm_chunk<NH, 0>(g, acc, 0, 0, 0);
  WAIT_BAR(0);
  STAMP(4);
  gemm_chunk<NH, 0>(g, acc, 1, 0, 0);
  STAMP(5);
  const int sp = w & 1, kq = l >> 4;
  constexpr int NW = (NH == 0) ? 3 : 2;
#pragma unroll
  for (int a = 0; a < 2; ++a) {
    const int sl = 32 * sp + 16 * a + (l & 15);
    const float gt = *(const LDS_AS float*)(size_t)(lds0 + O_G + sl * 4);
    const float og = rbf(1.0f - gt);
    const int zp = (sl & ~7) + ((sl & 1) << 2) + ((sl & 7) >> 1);
#pragma unroll
    for (int t = 0; t < NW; ++t) {
      const int n = 48 * NH + 16 * t + 4 * kq;
      const u32x2 xv = *(const LDS_AS u32x2*)(size_t)(lds0 + O_X + sl * 160 + n * 2);
      float r[4];
#pragma unroll
      for (int e = 0; e < 4; ++e) {
        const float z = __uint_as_float((unsigned)(*(const LDS_AS u16*)(size_t)(lds0 + O_Z + (n + e) * 128 + zp * 2)) << 16);
        const float xq = (e & 1) ? bf_hi(xv[e >> 1]) : bf_lo(xv[e >> 1]);
        const float av = rbf(xq * z);
        const float bv = rbf(acc[a][t][e]);
        r[e] = rbf(gt * av) + rbf(og * bv);
      }
      *(u32x2*)(out + (size_t)(s0 + sl) * 1280 + (hg * 80 + n) * 2) = u32x2{cvtpk(r[0], r[1]), cvtpk(r[2], r[3])};
    }
  }
  STAMP(6); WAIT_VM(0); STAMP(7);
  STAMP_FLUSH(2);
}

__global__ void __launch_bounds__(256, 1)
k_out(const char* __restrict__ x, const char* __restrict__ wo, char* __restrict__ ws, char* __restrict__ out) {
  extern __shared__ __attribute__((aligned(16))) char smem[];
  const unsigned lds0 = (unsigned)(size_t)(lds_ptr)smem;
  const int bid = blockIdx.x;
  const int qt = (bid & 7) * 4 + ((bid >> 3) & 3), hg = bid >> 5;
  const int l = threadIdx.x & 63;
  const int w = __builtin_amdgcn_readfirstlane(threadIdx.x >> 6);
  GemmLane g;
  gemm_setup(g, w, l, lds0);
  if (w < 2) out_body<0>(x, wo, ws, out, qt, hg, w, l, lds0, g);
  else out_body<1>(x, wo, ws, out, qt, hg, w, l, lds0, g);
}

extern "C" void kernel_launch(void* const* d_in, const int* in_sizes, int n_in, void* d_out, int out_size, void* d_ws,
                              size_t ws_size, hipStream_t stream) {
  const char* x = (const char*)d_in[0];
  const char* kin = (const char*)d_in[1];
  const char* vin = (const char*)d_in[2];
  const char* shk = (const char*)d_in[3];
  const char* hk = (const char*)d_in[4];
  const char* sd = (const char*)d_in[5];
  const char* wq = (const char*)d_in[7];
  const char* wk = (const char*)d_in[8];
  const char* wv = (const char*)d_in[9];
  const char* wo = (const char*)d_in[10];
  const char* gw = (const char*)d_in[11];
  char* ws = (char*)d_ws;
  (void)hipFuncSetAttribute((const void*)k_prep, hipFuncAttributeMaxDynamicSharedMemorySize, P_BYTES);
  (void)hipFuncSetAttribute((const void*)k_main, hipFuncAttributeMaxDynamicSharedMemorySize, M_BYTES);
  (void)hipFuncSetAttribute((const void*)k_out, hipFuncAttributeMaxDynamicSharedMemorySize, O_BYTES);
  k_prep<<<256, 256, P_BYTES, stream>>>(x, kin, vin, shk, wq, wk, wv, gw, ws);
  k_main<<<256, 768, M_BYTES, stream>>>(hk, sd, ws);
  k_out<<<256, 256, O_BYTES, stream>>>(x, wo, ws, (char*)d_out);
}
```

```cpp
#define PROBING 1
#include <hip/hip_runtime.h>
#include <hip/hip_bf16.h>
#include <stdint.h>

typedef __attribute__((ext_vector_type(8))) __bf16 bf16x8;
typedef __attribute__((ext_vector_type(4))) __bf16 bf16x4;
typedef __attribute__((ext_vector_type(16))) __bf16 bf16x16;
typedef __attribute__((ext_vector_type(4))) short s16x4;
typedef __attribute__((ext_vector_type(16))) float f32x16;
typedef __attribute__((ext_vector_type(8))) float f32x8;
typedef __attribute__((ext_vector_type(4))) float f32x4;
typedef __attribute__((ext_vector_type(4))) unsigned u32x4;
typedef __attribute__((ext_vector_type(2))) unsigned u32x2;
typedef unsigned short u16;
#define LDS_AS __attribute__((address_space(3)))
typedef LDS_AS char* lds_ptr;

constexpr int S = 2048, D = 640;
constexpr float QSCALE = 0.22360679774997896f * 1.4426950408889634f;

constexpr size_t WS_QP = 0;
constexpr size_t WS_O = WS_QP + (size_t)S * D * 2;
constexpr size_t WS_ZT = WS_O + (size_t)S * D * 2;
constexpr size_t WS_UT = WS_ZT + (size_t)S * D * 2;
constexpr size_t WS_KV = WS_UT + (size_t)S * D * 4;
constexpr size_t WS_KN = WS_KV + 64 * 4096;
constexpr size_t WS_G = WS_KN + 1024;

#define STAMP_DECL
#define STAMP(i)
#define STAMP_FLUSH(k)
#define PB(k) PROBE_BEGIN(k)
#define PE(k) PROBE_END(k)
#define PROBE_NW 4, 12, 4

#define PR_OFF (96u<<20)
#define PROBE_BEGIN(k) if (threadIdx.x == 0) { unsigned long long r_; asm volatile("s_memrealtime %0\n\ts_waitcnt lgkmcnt(0)" : "=s"(r_) :: "memory"); ((unsigned long long*)(ws + PR_OFF))[((k) * 256 + blockIdx.x) * 2] = r_; }
#define PROBE_END(k) { asm volatile("s_waitcnt vmcnt(0)" ::: "memory"); if ((threadIdx.x & 63) == 0) { unsigned long long r_; asm volatile("s_memrealtime %0\n\ts_waitcnt lgkmcnt(0)" : "=s"(r_) :: "memory"); ((unsigned long long*)(ws + PR_OFF + (1u<<20)))[((k) * 256 + blockIdx.x) * 16 + (threadIdx.x >> 6)] = r_; } }
#define SBAR() __builtin_amdgcn_sched_barrier(0)
#define PIN(x) asm volatile("" : "+v"(x))
#define WAIT_VM(N) asm volatile("s_waitcnt vmcnt(" #N ")" ::: "memory")
#define WAIT_BAR(N) asm volatile("s_waitcnt vmcnt(" #N ") lgkmcnt(0)\n\ts_barrier" ::: "memory")
#define BAR_ONLY() asm volatile("s_waitcnt lgkmcnt(0)\n\ts_barrier" ::: "memory")

__device__ __forceinline__ unsigned cvtpk(float lo, float hi) {
  unsigned r;
  asm("v_cvt_pk_bf16_f32 %0, %1, %2" : "=v"(r) : "v"(lo), "v"(hi));
  return r;
}
__device__ __forceinline__ float bf_lo(unsigned w) { return __uint_as_float(w << 16); }
__device__ __forceinline__ float bf_hi(unsigned w) { return __uint_as_float(w & 0xffff0000u); }
__device__ __forceinline__ float rbf(float x) { return bf_lo(cvtpk(x, x)); }

__device__ __forceinline__ void glds16(const void* base, unsigned off, unsigned lds) {
  unsigned sv;
  asm volatile("s_mov_b32 %0, m0\n\ts_mov_b32 m0, %3\n\ts_nop 0\n\tglobal_load_lds_dwordx4 %1, %2\n\ts_mov_b32 m0, %0"
               : "=&s"(sv) : "v"(off), "s"(base), "s"(lds) : "memory");
}
__device__ __forceinline__ void gstore16(void* base, unsigned off, u32x4 v) {
  asm volatile("global_store_dwordx4 %0, %1, %2\n\ts_nop 1" :: "v"(off), "v"(v), "s"(base) : "memory");
}
__device__ __forceinline__ bf16x8 lds_b128(lds_ptr p) { return *(const LDS_AS bf16x8*)p; }
__device__ __forceinline__ s16x4 lds_tr(lds_ptr p) {
  return __builtin_amdgcn_ds_read_tr16_b64_v4i16((LDS_AS s16x4*)p);
}
__device__ __forceinline__ bf16x8 cat4(s16x4 a, s16x4 b) {
  return __builtin_bit_cast(bf16x8, __builtin_shufflevector(a, b, 0, 1, 2, 3, 4, 5, 6, 7));
}
#define MFMA16(a, b, c) __builtin_amdgcn_mfma_f32_16x16x32_bf16(a, b, c, 0, 0, 0)

constexpr int G_XB = 0;
constexpr int G_WB = G_XB + 3 * 16384;
constexpr int G_END = G_WB + 3 * 20480;

struct GemmLane {
  unsigned xoff[4], woff[5];
  unsigned xa[4];
  unsigned wa0, wa1;
};

__device__ __forceinline__ void gemm_setup(GemmLane& g, int w, int l, unsigned lds0) {
#pragma unroll
  for (int i = 0; i < 4; ++i) {
    const int r = 4 * i + (l >> 4);
    g.xoff[i] = (16 * w + r) * 1280 + 16 * ((l & 15) ^ r);
  }
#pragma unroll
  for (int i = 0; i < 5; ++i) {
    const int n = 320 * w + 64 * i + l;
    const int pr = n / 10, cc = n - pr * 10;
    const int k = pr ^ (((pr >> 3) & 1) << 2);
    g.woff[i] = k * 1280 + 16 * cc;
  }
  const int sp = w & 1, nh = w >> 1;
  const int r = l & 15, kq = l >> 4;
#pragma unroll
  for (int ks = 0; ks < 4; ++ks) g.xa[ks] = lds0 + G_XB + (32 * sp + r) * 256 + 16 * ((4 * ks + kq) ^ r);
  const int q = (l & 15) >> 2, p = l & 3;
  g.wa0 = lds0 + G_WB + (8 * kq + 4 * (kq & 1) + q) * 160 + 8 * p + 96 * nh;
  g.wa1 = lds0 + G_WB + (8 * kq + 4 * (1 - (kq & 1)) + q) * 160 + 8 * p + 96 * nh;
}
__device__ __forceinline__ void gemm_issue(const GemmLane& g, const char* abase, const char* wbase, int kc, int buf,
                                           int w, unsigned lds0) {
  const char* ab = abase + kc * 256;
  const char* wb = wbase + (size_t)kc * 128 * 1280;
#pragma unroll
  for (int i = 0; i < 4; ++i) glds16(ab, g.xoff[i], lds0 + G_XB + buf * 16384 + (4 * w + i) * 1024);
#pragma unroll
  for (int i = 0; i < 5; ++i) glds16(wb, g.woff[i], lds0 + G_WB + buf * 20480 + (5 * w + i) * 1024);
}
template <int NH, int EXMODE>
__device__ __forceinline__ void gemm_chunk(const GemmLane& g, f32x4 (&acc)[2][3], int buf, unsigned exa0,
                                           unsigned exa1) {
#pragma unroll
  for (int ks = 0; ks < 4; ++ks) {
    bf16x8 xf[2], wf[3];
#pragma unroll
    for (int a = 0; a < 2; ++a) xf[a] = lds_b128((lds_ptr)(size_t)(g.xa[ks] + buf * 16384 + a * 4096));
    constexpr int NW = (NH == 0) ? 3 : 2;
#pragma unroll
    for (int t = 0; t < NW; ++t)
      wf[t] = cat4(lds_tr((lds_ptr)(size_t)(g.wa0 + buf * 20480 + ks * 5120 + t * 32)),
                   lds_tr((lds_ptr)(size_t)(g.wa1 + buf * 20480 + ks * 5120 + t * 32)));
    if (NH == 1 && EXMODE != 0)
      wf[2] = cat4(lds_tr((lds_ptr)(size_t)(exa0 + ks * 1024)), lds_tr((lds_ptr)(size_t)(exa1 + ks * 1024)));
#pragma unroll
    for (int a = 0; a < 2; ++a) {
#pragma unroll
      for (int t = 0; t < NW; ++t) acc[a][t] = MFMA16(wf[t], xf[a], acc[a][t]);
      if (NH == 1 && EXMODE == 1) acc[a][2] = MFMA16(wf[2], xf[a], acc[a][2]);
      if (NH == 1 && EXMODE == 2) acc[a][2] = MFMA16(xf[a], wf[2], acc[a][2]);
    }
  }
}

constexpr int P_EX = G_END;
constexpr int P_KV = P_EX + 20480;
constexpr int P_GW = P_KV + 4 * 6144;
constexpr int P_SK = P_GW + 2048;
constexpr int P_BYTES = P_SK + 1024;

template <int NH, int EXMODE>
__device__ __forceinline__ void prep_body(const char* x, const char* kin, const char* vin, const char* wq, char* ws,
                                          int qt, int hg, int w, int l, unsigned lds0, const GemmLane& g) {
  STAMP_DECL; STAMP(0);
  const int s0 = qt * 64;
  const char* abase = x + (size_t)s0 * 1280;
  const char* wbase = wq + hg * 160;
  gemm_issue(g, abase, wbase, 0, 0, w, lds0);
  gemm_issue(g, abase, wbase, 1, 1, w, lds0);
  gemm_issue(g, abase, wbase, 2, 2, w, lds0);
  {
    const int sw = s0 + 16 * w;
#pragma unroll
    for (int i = 0; i < 6; ++i) {
      int n = 64 * i + l;
      n = n < 370 ? n : 369;
      const bool isk = n < 200;
      const int m = isk ? n : n - 200;
      const int rr = m / 10, cc = m - rr * 10;
      int row = sw + rr - (isk ? 4 : 1);
      row = row < 0 ? 0 : row;
      const unsigned off = row * 1280 + hg * 160 + cc * 16;
      if (i < 3) glds16(kin, off, lds0 + P_KV + w * 6144 + i * 1024);
      else if (i > 3) glds16(vin, off, lds0 + P_KV + w * 6144 + i * 1024);
      else {
        const char* b = kin;
        const long long dv = (long long)(vin - kin);
        (void)b; (void)dv;
        if (isk) glds16(kin, off, lds0 + P_KV + w * 6144 + i * 1024);
        if (!isk) glds16(vin, off, lds0 + P_KV + w * 6144 + i * 1024);
      }
    }
  }
  f32x4 acc[2][3];
#pragma unroll
  for (int a = 0; a < 2; ++a)
#pragma unroll
    for (int t = 0; t < 3; ++t) acc[a][t] = f32x4{0.f, 0.f, 0.f, 0.f};
  const int kq = l >> 4, q4 = (l & 15) >> 2, p4 = l & 3;
  const unsigned exa0 = lds0 + P_EX + (8 * kq + 4 * (kq & 1) + q4) * 32 + 8 * p4;
  const unsigned exa1 = lds0 + P_EX + (8 * kq + 4 * (1 - (kq & 1)) + q4) * 32 + 8 * p4;
  const int grow = 8 * hg + 2 * w + (l >> 5), gi = l & 31;
  const unsigned gxa = lds0 + G_XB + grow * 256 + 16 * ((gi >> 1) ^ (grow & 15)) + 8 * (gi & 1);
  const unsigned gwa = lds0 + P_GW + gi * 8;
  float gacc = 0.f;
#define GATE(kc, buf)                                                                   \
  {                                                                                     \
    const u32x2 xv = *(const LDS_AS u32x2*)(size_t)(gxa + (buf) * 16384);               \
    const u32x2 wv = *(const LDS_AS u32x2*)(size_t)(gwa + (kc) * 256);                  \
    const unsigned p0 = cvtpk(bf_lo(xv[0]) * bf_lo(wv[0]), bf_hi(xv[0]) * bf_hi(wv[0])); \
    const unsigned p1 = cvtpk(bf_lo(xv[1]) * bf_lo(wv[1]), bf_hi(xv[1]) * bf_hi(wv[1])); \
    gacc += (bf_lo(p0) + bf_hi(p0)) + (bf_lo(p1) + bf_hi(p1));                          \
  }
  STAMP(1);
  WAIT_BAR(25);
  STAMP(2);
  gemm_chunk<NH, EXMODE>(g, acc, 0, exa0, exa1);
  GATE(0, 0);
  WAIT_BAR(16);
  gemm_issue(g, abase, wbase, 3, 0, w, lds0);
  gemm_chunk<NH, EXMODE>(g, acc, 1, exa0 + 4096, exa1 + 4096);
  GATE(1, 1);
  WAIT_BAR(16);
  gemm_issue(g, abase, wbase, 4, 1, w, lds0);
  STAMP(3);
  WAIT_VM(18);
  {
    const int dp = l < 40 ? l : 39;
    const unsigned ka = lds0 + P_KV + w * 6144 + dp * 4;
    const u32x4 sk = *(const LDS_AS u32x4*)(size_t)(lds0 + P_SK + dp * 16);
    const float a0 = bf_lo(sk[0]), a1 = bf_hi(sk[0]), a2 = bf_lo(sk[1]), a3 = bf_hi(sk[1]);
    const float b0 = bf_lo(sk[2]), b1 = bf_hi(sk[2]), b2 = bf_lo(sk[3]), b3 = bf_hi(sk[3]);
    char* ut = ws + WS_UT;
#pragma unroll
    for (int oc = 0; oc < 2; ++oc) {
      const int so = s0 + 16 * w + 8 * oc;
      float kl[12], kh[12];
#pragma unroll
      for (int j = 0; j < 12; ++j) {
        unsigned kk = *(const LDS_AS unsigned*)(size_t)(ka + (8 * oc + j) * 160);
        if (j < 4) kk = (so == 0) ? 0u : kk;
        kl[j] = bf_lo(kk);
        kh[j] = bf_hi(kk);
      }
      float ul[9], uh[9];
#pragma unroll
      for (int j = 0; j < 9; ++j) {
        unsigned vv = *(const LDS_AS unsigned*)(size_t)(ka + 3200 + (8 * oc + j) * 160);
        if (j == 0) vv = (so == 0) ? 0u : vv;
        const float sl = fmaf(kl[j + 3], a0, fmaf(kl[j + 2], a1, fmaf(kl[j + 1], a2, kl[j] * a3)));
        const float sh = fmaf(kh[j + 3], b0, fmaf(kh[j + 2], b1, fmaf(kh[j + 1], b2, kh[j] * b3)));
        ul[j] = sl * bf_lo(vv);
        uh[j] = sh * bf_hi(vv);
      }
      const unsigned pos = (2040 - so) * 2;
      const unsigned ch = (hg * 80 + 2 * dp) * 8192;
      const u32x4 l1 = {cvtpk(ul[8], ul[7]), cvtpk(ul[6], ul[5]), cvtpk(ul[4], ul[3]), cvtpk(ul[2], ul[1])};
      const u32x4 l0 = {cvtpk(ul[7], ul[6]), cvtpk(ul[5], ul[4]), cvtpk(ul[3], ul[2]), cvtpk(ul[1], ul[0])};
      const u32x4 h1 = {cvtpk(uh[8], uh[7]), cvtpk(uh[6], uh[5]), cvtpk(uh[4], uh[3]), cvtpk(uh[2], uh[1])};
      const u32x4 h0 = {cvtpk(uh[7], uh[6]), cvtpk(uh[5], uh[4]), cvtpk(uh[3], uh[2]), cvtpk(uh[1], uh[0])};
      if (l < 40) {
        gstore16(ut, ch + pos, l0);
        gstore16(ut, ch + 4096 + pos, l1);
        gstore16(ut, ch + 8192 + pos, h0);
        gstore16(ut, ch + 8192 + 4096 + pos, h1);
      }
    }
  }
  STAMP(4);
  gemm_chunk<NH, EXMODE>(g, acc, 2, exa0 + 8192, exa1 + 8192);
  GATE(2, 2);
  WAIT_BAR(17);
  gemm_chunk<NH, EXMODE>(g, acc, 0, exa0 + 12288, exa1 + 12288);
  GATE(3, 0);
  WAIT_BAR(8);
  gemm_chunk<NH, EXMODE>(g, acc, 1, exa0 + 16384, exa1 + 16384);
  GATE(4, 1);
#undef GATE
  STAMP(5);
  const int sp = w & 1;
  {
    constexpr int NW = (NH == 0) ? 3 : 2;
#pragma unroll
    for (int a = 0; a < 2; ++a)
#pragma unroll
      for (int t = 0; t < NW; ++t) {
        const int s = s0 + 32 * sp + 16 * a + (l & 15);
        const int n = hg * 80 + 48 * NH + 16 * t + 4 * kq;
        const u32x2 v = {cvtpk(acc[a][t][0] * QSCALE, acc[a][t][1] * QSCALE),
                         cvtpk(acc[a][t][2] * QSCALE, acc[a][t][3] * QSCALE)};
        *(u32x2*)(ws + WS_QP + (size_t)s * 1280 + n * 2) = v;
      }
  }
  {
    float t = gacc;
#pragma unroll
    for (int o = 1; o < 32; o <<= 1) t += __shfl_xor(t, o);
    const float z = rbf(t);
    const float sg = rbf(1.0f / (1.0f + __expf(-z)));
    if (gi == 0) ((float*)(ws + WS_G))[s0 + grow] = sg;
  }
  char* kvt = ws + WS_KV + (size_t)(2 * qt + sp) * 4096;
  if (NH == 1 && EXMODE != 0) {
#pragma unroll
    for (int a = 0; a < 2; ++a) {
      const u32x2 v = {cvtpk(acc[a][2][0], acc[a][2][1]), cvtpk(acc[a][2][2], acc[a][2][3])};
      if (EXMODE == 2) {
        *(u32x2*)(kvt + 1024 + 1024 * a + 16 * ((l & 15) + 32 * (kq >> 1)) + 8 * (kq & 1)) = v;
      } else {
        const int key = 16 * a + (l & 15);
        float nn = bf_lo(v[0]) * bf_lo(v[0]) + bf_hi(v[0]) * bf_hi(v[0]) + bf_lo(v[1]) * bf_lo(v[1]) +
                   bf_hi(v[1]) * bf_hi(v[1]);
        if (hg == 0) {
          *(u32x2*)(kvt + 16 * (key + 32 * (kq >> 1)) + 8 * (kq & 1)) = v;
          nn += __shfl_xor(nn, 16);
          nn += __shfl_xor(nn, 32);
        } else {
          if (kq == 1) *(u32x2*)(kvt + 3072 + 8 * key) = v;
          if (kq == 3) {
            char* vb = kvt + 1024 + 1024 * a + 16 * (16 + 32 * ((key >> 3) & 1)) + 2 * (key & 7);
            *(u16*)(vb) = (u16)(v[0] & 0xffff);
            *(u16*)(vb + 16) = (u16)(v[0] >> 16);
            *(u16*)(vb + 32) = (u16)(v[1] & 0xffff);
            *(u16*)(vb + 48) = (u16)(v[1] >> 16);
          }
          nn = (kq == 1) ? nn : 0.f;
          nn = fmaxf(nn, __shfl_xor(nn, 16));
        }
#pragma unroll
        for (int o = 1; o < 16; o <<= 1) nn = fmaxf(nn, __shfl_xor(nn, o));
        if (l == 0) ((float*)(ws + WS_KN))[(hg == 0 ? 0 : 128) + qt * 4 + 2 * sp + a] = nn;
      }
    }
  }
  if (hg == 3 && NH == 0) {
    if (l < 32) *(u32x2*)(kvt + 3072 + 256 + 8 * l) = u32x2{0x3f803f80u, 0x00003f80u};
    if (l < 48) {
      const int d = 20 + (l % 12), q = l / 12;
      const unsigned c = (d == 20 || d == 24) ? 0x3f803f80u : 0u;
      *(u32x4*)(kvt + 1024 + 1024 * (q >> 1) + 16 * (d + 32 * (q & 1))) = u32x4{c, c, c, c};
    }
  }
  STAMP(6); WAIT_VM(0); STAMP(7);
  STAMP_FLUSH(0);
}

__global__ void __launch_bounds__(256, 1)
k_prep(const char* __restrict__ x, const char* __restrict__ kin, const char* __restrict__ vin,
       const char* __restrict__ shk, const char* __restrict__ wq, const char* __restrict__ wk,
       const char* __restrict__ wv, const char* __restrict__ gw, char* __restrict__ ws) {
  extern __shared__ __attribute__((aligned(16))) char smem[];
  const unsigned lds0 = (unsigned)(size_t)(lds_ptr)smem;
  const int bid = blockIdx.x;
  const int qt = (bid & 7) * 4 + ((bid >> 3) & 3), hg = bid >> 5;
  const int l = threadIdx.x & 63;
  const int w = __builtin_amdgcn_readfirstlane(threadIdx.x >> 6);
  PB(0);
  if (hg < 3) {
#pragma unroll
    for (int i = 0; i < 5; ++i) {
      const int n = 320 * w + 64 * i + l;
      const int pr = n >> 1, cc = n & 1;
      const int k = pr ^ (((pr >> 3) & 1) << 2);
      const unsigned dst = lds0 + P_EX + (5 * w + i) * 1024;
      if (hg == 0) glds16(wk, k * 40 + 16 * cc, dst);
      else if (hg == 1) glds16(wv, k * 40 + 16 * cc, dst);
      else {
        if (cc == 0) glds16(wk, k * 40 + 24, dst);
        if (cc == 1) glds16(wv, k * 40 + 24, dst);
      }
    }
  }
  if (w == 0) glds16(gw, l * 16, lds0 + P_GW);
  if (w == 1 && l < 16) glds16(gw, 1024 + l * 16, lds0 + P_GW + 1024);
  if (w == 2 && l < 40) glds16(shk, hg * 640 + l * 16, lds0 + P_SK);
  GemmLane g;
  gemm_setup(g, w, l, lds0);
  if (w < 2) prep_body<0, 0>(x, kin, vin, wq, ws, qt, hg, w, l, lds0, g);
  else if (hg == 1) prep_body<1, 2>(x, kin, vin, wq, ws, qt, hg, w, l, lds0, g);
  else if (hg < 3) prep_body<1, 1>(x, kin, vin, wq, ws, qt, hg, w, l, lds0, g);
  else prep_body<1, 0>(x, kin, vin, wq, ws, qt, hg, w, l, lds0, g);
  PE(0);
}

constexpr int M_RING = 0;
constexpr int CV_H = 4160, CV_U = 4640, CV_CH = 4 * CV_H + 2 * CV_U;
constexpr int M_CV = 65536;
constexpr int M_BYTES = M_CV + 3 * CV_CH;

#define MFMA32(a, b, c) __builtin_amdgcn_mfma_f32_32x32x16_bf16(a, b, c, 0, 0, 0)
#define MFMA8(a, b, c) __builtin_amdgcn_mfma_f32_32x32x8bf16_1k(a, b, c, 0, 0, 0)
__device__ __forceinline__ f32x16 pv_lo(bf16x8 v, f32x16 p, f32x16 o, int idx) {
  const f32x8 h = __builtin_shufflevector(p, p, 0, 1, 2, 3, 4, 5, 6, 7);
  return __builtin_amdgcn_smfmac_f32_32x32x32_bf16(v, __builtin_bit_cast(bf16x16, h), o, idx, 0, 0);
}
__device__ __forceinline__ f32x16 pv_hi(bf16x8 v, f32x16 p, f32x16 o, int idx) {
  const f32x8 h = __builtin_shufflevector(p, p, 8, 9, 10, 11, 12, 13, 14, 15);
  return __builtin_amdgcn_smfmac_f32_32x32x32_bf16(v, __builtin_bit_cast(bf16x16, h), o, idx, 0, 0);
}
#define EX2(v) __builtin_amdgcn_exp2f(v)

__device__ __forceinline__ void attn_wave(char* ws, int qt, int hg, int w, int l, unsigned lds0) {
  STAMP_DECL; STAMP(0);
  const char* kvg = ws + WS_KV;
  const int h = l >> 5;
  const int head = hg * 4 + (w & 3);
  const int srow = qt * 64 + 32 * (w >> 2) + (l & 31);
  const unsigned dsrc = w * 2048 + l * 16;
  const unsigned ddst = lds0 + M_RING + w * 2048;
#define RING_ISSUE(c)                                                            \
  {                                                                              \
    const char* b_ = kvg + ((c) & 15) * 16384;                                   \
    glds16(b_, dsrc, ddst + ((c) & 3) * 16384);                                  \
    glds16(b_, dsrc + 1024, ddst + ((c) & 3) * 16384 + 1024);                    \
  }
  const char* qrow = ws + WS_QP + (size_t)srow * 1280 + head * 40;
  u32x4 q16 = *(const u32x4*)(qrow + 16 * h);
  u32x2 q8 = *(const u32x2*)(qrow + 32);
  const f32x4 kn4 = *(const f32x4*)(ws + WS_KN + (h * 128 + (l & 31) * 4) * 4);
  RING_ISSUE(0);
  RING_ISSUE(1);
  RING_ISSUE(2);
  float kn = fmaxf(fmaxf(kn4[0], kn4[1]), fmaxf(kn4[2], kn4[3]));
#pragma unroll
  for (int o = 1; o < 32; o <<= 1) kn = fmaxf(kn, __shfl_xor(kn, o));
  kn += __shfl_xor(kn, 32);
  float qn = 0.f;
#pragma unroll
  for (int i = 0; i < 4; ++i) qn += bf_lo(q16[i]) * bf_lo(q16[i]) + bf_hi(q16[i]) * bf_hi(q16[i]);
  if (h == 0) {
#pragma unroll
    for (int i = 0; i < 2; ++i) qn += bf_lo(q8[i]) * bf_lo(q8[i]) + bf_hi(q8[i]) * bf_hi(q8[i]);
  }
  qn += __shfl_xor(qn, 32);
  {
    const float mref = sqrtf(qn * kn) * 1.01f - 64.0f;
    const unsigned nb = cvtpk(-mref, 0.f);
    if (h == 1) q8 = u32x2{nb, 0u};
  }
  const bf16x8 qa = __builtin_bit_cast(bf16x8, q16);
  s16x4 qb = __builtin_bit_cast(s16x4, q8);
  const int idx = 0xdddd;
  const unsigned ra = lds0 + M_RING + l * 16, rb = lds0 + M_RING + 3072 + l * 8;
#define LK16(c, t) lds_b128((lds_ptr)(size_t)(ra + (c) + (t) * 4096))
#define LK8(c, t) (*(const LDS_AS s16x4*)(size_t)(rb + (c) + (t) * 4096))
#define LV0(c, t) lds_b128((lds_ptr)(size_t)(ra + (c) + (t) * 4096 + 1024))
#define LV1(c, t) lds_b128((lds_ptr)(size_t)(ra + (c) + (t) * 4096 + 2048))
  const f32x16 zero = {0.f, 0.f, 0.f, 0.f, 0.f, 0.f, 0.f, 0.f, 0.f, 0.f, 0.f, 0.f, 0.f, 0.f, 0.f, 0.f};
  f32x16 O = zero, SA, SB;
  STAMP(1);
  WAIT_BAR(4);
  STAMP(2);
  bf16x8 k16, v0, v1;
  s16x4 k8;
  k16 = LK16(0, 0);
  k8 = LK8(0, 0);
  SA = MFMA32(k16, qa, zero);
  SA = MFMA8(k8, qb, SA);
  k16 = LK16(0, 1);
  k8 = LK8(0, 1);
  v0 = LV0(0, 0);
  v1 = LV1(0, 0);
#define E4(X, i)                                                                           \
  X[i] = EX2(X[i]); X[i + 1] = EX2(X[i + 1]); X[i + 2] = EX2(X[i + 2]); X[i + 3] = EX2(X[i + 3]); \
  PIN(X); SBAR();
#define STEP(SC, SN, ck, tk, cv, tv)              \
  {                                               \
    SN = MFMA32(k16, qa, zero);                   \
    k16 = LK16(ck, tk);                           \
    E4(SC, 0);                                    \
    SN = MFMA8(k8, qb, SN);                       \
    k8 = LK8(ck, tk);                             \
    E4(SC, 4);                                    \
    E4(SC, 8);                                    \
    O = pv_lo(v0, SC, O, idx);                    \
    v0 = LV0(cv, tv);                             \
    E4(SC, 12);                                   \
    O = pv_hi(v1, SC, O, idx);                    \
    v1 = LV1(cv, tv);                             \
    SBAR();                                       \
  }
  for (int c = 0; c < 16; ++c) {
    const unsigned cur = (c & 3) * 16384, nxt = ((c + 1) & 3) * 16384;
    STEP(SA, SB, cur, 2, cur, 1);
    STEP(SB, SA, cur, 3, cur, 2);
    WAIT_BAR(2);
    RING_ISSUE(c + 3);
    STEP(SA, SB, nxt, 0, cur, 3);
    STEP(SB, SA, nxt, 1, nxt, 0);
  }
  STAMP(3);
  WAIT_VM(0);
  float lsum = h ? O[8] : O[12];
  float oabs = 0.f;
#pragma unroll
  for (int i = 0; i < 12; ++i) oabs += fabsf(O[i]);
  const bool ok = (lsum >= 9.094947e-13f) && (oabs < 3.0e38f);
  if (__builtin_expect(__any(!ok), 0)) {
    const char* ga = kvg + l * 16;
    const char* gb = kvg + 3072 + l * 8;
    if (h == 1) qb = s16x4{0, 0, 0, 0};
    float m = -INFINITY;
    for (int t = 0; t < 64; ++t) {
      f32x16 s = MFMA32(*(const bf16x8*)(ga + t * 4096), qa, zero);
      s = MFMA8(*(const s16x4*)(gb + t * 4096), qb, s);
#pragma unroll
      for (int i = 0; i < 16; ++i) m = fmaxf(m, s[i]);
    }
    m = fmaxf(m, __shfl_xor(m, 32));
    const float m0 = rbf(m), m1 = rbf(m - m0), m2 = rbf(m - m0 - m1);
    if (h == 1) qb = __builtin_bit_cast(s16x4, u32x2{cvtpk(-m0, -m1), cvtpk(-m2, 0.f)});
    O = zero;
    for (int t = 0; t < 64; ++t) {
      f32x16 s = MFMA32(*(const bf16x8*)(ga + t * 4096), qa, zero);
      s = MFMA8(*(const s16x4*)(gb + t * 4096), qb, s);
#pragma unroll
      for (int i = 0; i < 16; ++i) s[i] = EX2(s[i]);
      O = pv_lo(*(const bf16x8*)(ga + t * 4096 + 1024), s, O, idx);
      O = pv_hi(*(const bf16x8*)(ga + t * 4096 + 2048), s, O, idx);
    }
    lsum = h ? O[8] : O[12];
  }
  const float inv = 1.0f / lsum;
  char* orow = ws + WS_O + (size_t)srow * 1280 + head * 40 + 8 * h;
  *(u32x2*)(orow) = u32x2{cvtpk(O[0] * inv, O[1] * inv), cvtpk(O[2] * inv, O[3] * inv)};
  *(u32x2*)(orow + 16) = u32x2{cvtpk(O[4] * inv, O[5] * inv), cvtpk(O[6] * inv, O[7] * inv)};
  if (h == 0) *(u32x2*)(orow + 32) = u32x2{cvtpk(O[8] * inv, O[9] * inv), cvtpk(O[10] * inv, O[11] * inv)};
  STAMP(4); WAIT_VM(0); STAMP(5);
  STAMP_FLUSH(1);
}

__device__ __forceinline__ void conv_wave(const char* __restrict__ hk, const char* __restrict__ sd, char* ws,
                                          int bid, int j, int l, unsigned lds0) {
  STAMP_DECL; STAMP(0);
  const int nch = bid < 128 ? 3 : 2;
  const int ch0 = bid < 128 ? 3 * bid : 384 + 2 * (bid - 128);
  const unsigned cv0 = lds0 + M_CV;
  for (int c = 0; c < nch; ++c) {
    const char* hb = hk + (size_t)(ch0 + c) * 4096;
    const char* ub = ws + WS_UT + (size_t)(ch0 + c) * 8192;
#pragma unroll
    for (int i = 0; i < 4; ++i) {
      int e = 2 + 2 * j + 512 * i + 8 * l;
      e = e > 2040 ? 2040 : e;
      glds16(hb, e * 2, cv0 + c * CV_CH + j * CV_H + 64 + i * 1024);
    }
#pragma unroll
    for (int i = 0; i < 2; ++i)
      glds16(ub, (j >> 1) * 4096 + (j & 1) * 2048 + i * 1024 + l * 16,
             cv0 + c * CV_CH + 4 * CV_H + (j >> 1) * CV_U + (j & 1) * 2048 + i * 1024);
  }
  float dval[3];
  unsigned hv[3];
#pragma unroll
  for (int c = 0; c < 3; ++c) {
    const int cc = c < nch ? c : 0;
    const int e = 2 * (l & 15) - 30 + 2 * j;
    hv[c] = *(const unsigned*)(hk + (size_t)(ch0 + cc) * 4096 + (e < 0 ? 0 : e) * 2);
    dval[c] = __uint_as_float((unsigned)(*(const u16*)(sd + (ch0 + cc) * 2)) << 16);
  }
  BAR_ONLY();
  BAR_ONLY();
#pragma unroll
  for (int c = 0; c < 3; ++c) {
    const int e = 2 * (l & 15) - 30 + 2 * j;
    if (l < 16) *(LDS_AS unsigned*)(size_t)(cv0 + c * CV_CH + j * CV_H + l * 4) = e < 0 ? 0u : hv[c];
    if (j < 2 && l < 28) *(LDS_AS u32x4*)(size_t)(cv0 + c * CV_CH + 4 * CV_H + j * CV_U + 4096 + l * 16) = u32x4{0, 0, 0, 0};
  }
  STAMP(1);
  WAIT_BAR(0);
  STAMP(2);
  int bars = 14;
  const int i16 = l & 15, kq = l >> 4;
  const unsigned aa = cv0 + (i16 & 3) * CV_H + 16 * ((i16 >> 2) + kq);
  const unsigned ba = cv0 + 4 * CV_H + (i16 & 1) * CV_U + 2 * (2016 - 32 * (i16 >> 1) + 8 * kq);
  const int jj0 = (30 - 2 * (i16 & 3) - 8 * (i16 >> 2) - 8 * kq);
  int phase = 0;
  for (int c = 0; c < nch; ++c) {
    const unsigned db = __float_as_uint(c == 0 ? dval[0] : (c == 1 ? dval[1] : dval[2])) >> 16;
    const u32x4 dm = {jj0 == 0 ? db : 0u, jj0 == 2 ? db : 0u, jj0 == 4 ? db : 0u, jj0 == 6 ? db : 0u};
    const bf16x8 dfrag = __builtin_bit_cast(bf16x8, dm);
    for (int hi = 0; hi < 2; ++hi) {
      const int m = hi ? 7 - j : j;
      unsigned ap = aa + c * CV_CH, bp = ba + c * CV_CH - 512 * m;
      f32x4 acc = {0.f, 0.f, 0.f, 0.f};
      acc = MFMA16(dfrag, lds_b128((lds_ptr)(size_t)bp), acc);
      for (int gI = 0; gI <= m; ++gI) {
#pragma unroll
        for (int t = 0; t < 8; ++t)
          acc = MFMA16(lds_b128((lds_ptr)(size_t)(ap + 64 * t)), lds_b128((lds_ptr)(size_t)(bp + 64 * t)), acc);
        ap += 512;
        bp += 512;
        phase ^= 1;
        if (phase == 0 && bars > 0) {
          BAR_ONLY();
          --bars;
        }
      }
      *(u32x2*)(ws + WS_ZT + (size_t)(ch0 + c) * 4096 + (256 * m + 32 * (i16 >> 1) + 8 * kq + 4 * (i16 & 1)) * 2) =
          u32x2{cvtpk(acc[0], acc[1]), cvtpk(acc[2], acc[3])};
    }
  }
  STAMP(3); STAMP(4); WAIT_VM(0); STAMP(5);
  STAMP_FLUSH(1);
}

__global__ void __launch_bounds__(768, 3)
k_main(const char* __restrict__ hk, const char* __restrict__ sd, char* __restrict__ ws) {
  extern __shared__ __attribute__((aligned(16))) char smem[];
  const unsigned lds0 = (unsigned)(size_t)(lds_ptr)smem;
  const int bid = blockIdx.x;
  const int qt = (bid & 7) * 4 + ((bid >> 3) & 3), hg = bid >> 5;
  const int l = threadIdx.x & 63;
  const int w = __builtin_amdgcn_readfirstlane(threadIdx.x >> 6);
  PB(1);
  if (w < 8) attn_wave(ws, qt, hg, w, l, lds0);
  else conv_wave(hk, sd, ws, bid, w - 8, l, lds0);
  PE(1);
}

constexpr int O_Z = G_END;
constexpr int O_X = O_Z + 12288;
constexpr int O_G = O_X + 12288;
constexpr int O_BYTES = O_G + 4096;

template <int NH>
__device__ __forceinline__ void out_body(const char* x, const char* wo, char* ws, char* out, int qt, int hg, int w,
                                         int l, unsigned lds0, const GemmLane& g) {
  STAMP_DECL; STAMP(0);
  const int s0 = qt * 64;
  const char* abase = ws + WS_O + (size_t)s0 * 1280;
  const char* wbase = wo + hg * 160;
  gemm_issue(g, abase, wbase, 0, 0, w, lds0);
  gemm_issue(g, abase, wbase, 1, 1, w, lds0);
  gemm_issue(g, abase, wbase, 2, 2, w, lds0);
#pragma unroll
  for (int i = 0; i < 3; ++i) {
    int n = 192 * w + 64 * i + l;
    n = n < 640 ? n : 639;
    glds16(ws + WS_ZT, (hg * 80 + (n >> 3)) * 4096 + s0 * 2 + (n & 7) * 16, lds0 + O_Z + (3 * w + i) * 1024);
    const int rr = n / 10, cc = n - rr * 10;
    glds16(x, (s0 + rr) * 1280 + hg * 160 + cc * 16, lds0 + O_X + (3 * w + i) * 1024);
  }
  glds16(ws + WS_G, s0 * 4 + (l & 15) * 16, lds0 + O_G + w * 1024);
  f32x4 acc[2][3];
#pragma unroll
  for (int a = 0; a < 2; ++a)
#pragma unroll
    for (int t = 0; t < 3; ++t) acc[a][t] = f32x4{0.f, 0.f, 0.f, 0.f};
  STAMP(1);
  WAIT_BAR(25);
  STAMP(2);
  gemm_chunk<NH, 0>(g, acc, 0, 0, 0);
  WAIT_BAR(16);
  gemm_issue(g, abase, wbase, 3, 0, w, lds0);
  gemm_chunk<NH, 0>(g, acc, 1, 0, 0);
  WAIT_BAR(16);
  gemm_issue(g, abase, wbase, 4, 1, w, lds0);
  gemm_chunk<NH, 0>(g, acc, 2, 0, 0);
  STAMP(3);
  WAIT_BAR(9);
  gemm_chunk<NH, 0>(g, acc, 0, 0, 0);
  WAIT_BAR(0);
  STAMP(4);
  gemm_chunk<NH, 0>(g, acc, 1, 0, 0);
  STAMP(5);
  const int sp = w & 1, kq = l >> 4;
  constexpr int NW = (NH == 0) ? 3 : 2;
#pragma unroll
  for (int a = 0; a < 2; ++a) {
    const int sl = 32 * sp + 16 * a + (l & 15);
    const float gt = *(const LDS_AS float*)(size_t)(lds0 + O_G + sl * 4);
    const float og = rbf(1.0f - gt);
    const int zp = (sl & ~7) + ((sl & 1) << 2) + ((sl & 7) >> 1);
#pragma unroll
    for (int t = 0; t < NW; ++t) {
      const int n = 48 * NH + 16 * t + 4 * kq;
      const u32x2 xv = *(const LDS_AS u32x2*)(size_t)(lds0 + O_X + sl * 160 + n * 2);
      float r[4];
#pragma unroll
      for (int e = 0; e < 4; ++e) {
        const float z = __uint_as_float((unsigned)(*(const LDS_AS u16*)(size_t)(lds0 + O_Z + (n + e) * 128 + zp * 2)) << 16);
        const float xq = (e & 1) ? bf_hi(xv[e >> 1]) : bf_lo(xv[e >> 1]);
        const float av = rbf(xq * z);
        const float bv = rbf(acc[a][t][e]);
        r[e] = rbf(gt * av) + rbf(og * bv);
      }
      *(u32x2*)(out + (size_t)(s0 + sl) * 1280 + (hg * 80 + n) * 2) = u32x2{cvtpk(r[0], r[1]), cvtpk(r[2], r[3])};
    }
  }
  STAMP(6); WAIT_VM(0); STAMP(7);
  STAMP_FLUSH(2);
}

__global__ void __launch_bounds__(256, 1)
k_out(const char* __restrict__ x, const char* __restrict__ wo, char* __restrict__ ws, char* __restrict__ out) {
  extern __shared__ __attribute__((aligned(16))) char smem[];
  const unsigned lds0 = (unsigned)(size_t)(lds_ptr)smem;
  const int bid = blockIdx.x;
  const int qt = (bid & 7) * 4 + ((bid >> 3) & 3), hg = bid >> 5;
  const int l = threadIdx.x & 63;
  const int w = __builtin_amdgcn_readfirstlane(threadIdx.x >> 6);
  GemmLane g;
  PB(2);
  gemm_setup(g, w, l, lds0);
  if (w < 2) out_body<0>(x, wo, ws, out, qt, hg, w, l, lds0, g);
  else out_body<1>(x, wo, ws, out, qt, hg, w, l, lds0, g);
  PE(2);
}

__global__ void k_delay(char* ws, int which, int nw0, int nw1, int nw2) {
  __shared__ unsigned long long mn[3][256], mx[3][256];
  const int t = threadIdx.x; const int nw[3] = {nw0, nw1, nw2};
  for (int k = 0; k < 3; ++k) { mn[k][t] = ((unsigned long long*)(ws + PR_OFF))[(k * 256 + t) * 2]; unsigned long long m = 0;
    for (int w = 0; w < nw[k]; ++w) { unsigned long long v = ((unsigned long long*)(ws + PR_OFF + (1u<<20)))[(k * 256 + t) * 16 + w]; m = v > m ? v : m; } mx[k][t] = m; }
  __syncthreads();
  if (t == 0) { unsigned long long a[3], b[3];
    for (int k = 0; k < 3; ++k) { a[k] = ~0ull; b[k] = 0; for (int i = 0; i < 256; ++i) { a[k] = mn[k][i] < a[k] ? mn[k][i] : a[k]; b[k] = mx[k][i] > b[k] ? mx[k][i] : b[k]; } }
    unsigned long long iv = 0;
    if (which == 1) iv = b[0] - a[0]; if (which == 2) iv = a[1] - b[0]; if (which == 3) iv = b[1] - a[1]; if (which == 4) iv = a[2] - b[1]; if (which == 5) iv = b[2] - a[2];
    if (iv > 10000) iv = 10000;
    unsigned long long r0, r; asm volatile("s_memrealtime %0\n\ts_waitcnt lgkmcnt(0)" : "=s"(r0) :: "memory");
    do { asm volatile("s_memrealtime %0\n\ts_waitcnt lgkmcnt(0)" : "=s"(r) :: "memory"); } while (r - r0 < iv * 10);
  }
}
extern "C" void kernel_launch(void* const* d_in, const int* in_sizes, int n_in, void* d_out, int out_size, void* d_ws,
                              size_t ws_size, hipStream_t stream) {
  const char* x = (const char*)d_in[0];
  const char* kin = (const char*)d_in[1];
  const char* vin = (const char*)d_in[2];
  const char* shk = (const char*)d_in[3];
  const char* hk = (const char*)d_in[4];
  const char* sd = (const char*)d_in[5];
  const char* wq = (const char*)d_in[7];
  const char* wk = (const char*)d_in[8];
  const char* wv = (const char*)d_in[9];
  const char* wo = (const char*)d_in[10];
  const char* gw = (const char*)d_in[11];
  char* ws = (char*)d_ws;
  (void)hipFuncSetAttribute((const void*)k_prep, hipFuncAttributeMaxDynamicSharedMemorySize, P_BYTES);
  (void)hipFuncSetAttribute((const void*)k_main, hipFuncAttributeMaxDynamicSharedMemorySize, M_BYTES);
  (void)hipFuncSetAttribute((const void*)k_out, hipFuncAttributeMaxDynamicSharedMemorySize, O_BYTES);
  k_prep<<<256, 256, P_BYTES, stream>>>(x, kin, vin, shk, wq, wk, wv, gw, ws);
  k_main<<<256, 768, M_BYTES, stream>>>(hk, sd, ws);
  k_out<<<256, 256, O_BYTES, stream>>>(x, wo, ws, (char*)d_out);
  k_delay<<<1,256,0,stream>>>(ws, 0, PROBE_NW);
}
```

```cpp
#include <hip/hip_runtime.h>
#include <hip/hip_bf16.h>
#include <stdint.h>

typedef __attribute__((ext_vector_type(8))) __bf16 bf16x8;
typedef __attribute__((ext_vector_type(4))) __bf16 bf16x4;
typedef __attribute__((ext_vector_type(16))) __bf16 bf16x16;
typedef __attribute__((ext_vector_type(4))) short s16x4;
typedef __attribute__((ext_vector_type(16))) float f32x16;
typedef __attribute__((ext_vector_type(8))) float f32x8;
typedef __attribute__((ext_vector_type(4))) float f32x4;
typedef __attribute__((ext_vector_type(4))) unsigned u32x4;
typedef __attribute__((ext_vector_type(2))) unsigned u32x2;
typedef unsigned short u16;
#define LDS_AS __attribute__((address_space(3)))
typedef LDS_AS char* lds_ptr;

constexpr int S = 2048, D = 640;
constexpr float QSCALE = 0.22360679774997896f * 1.4426950408889634f;

constexpr size_t WS_QF = 0;
constexpr size_t WS_OF = WS_QF + 256 * 12288;
constexpr size_t WS_ZT = WS_OF + (size_t)S * D * 2;
constexpr size_t WS_UT = WS_ZT + (size_t)S * D * 2;
constexpr size_t WS_WF = WS_UT + (size_t)S * D * 2;
constexpr size_t WS_KV = WS_WF + (size_t)D * D * 2;
constexpr size_t WS_G = WS_KV + 32 * 7168;

#define STAMP_DECL
#define STAMP(i)
#define STAMP_FLUSH(k)
#define PB(k)
#define PE(k)
#define TWAIT(X) X
#define SBAR() __builtin_amdgcn_sched_barrier(0)
#define PIN(x) asm volatile("" : "+v"(x))
#define WAIT_VM(N) asm volatile("s_waitcnt vmcnt(" #N ")" ::: "memory")
#define WAIT_BAR(N) asm volatile("s_waitcnt vmcnt(" #N ") lgkmcnt(0)\n\ts_barrier" ::: "memory")
#define WAITVM_BAR(N) asm volatile("s_waitcnt vmcnt(" #N ")\n\ts_barrier" ::: "memory")
#define BAR_ONLY() asm volatile("s_waitcnt lgkmcnt(0)\n\ts_barrier" ::: "memory")

__device__ __forceinline__ unsigned cvtpk(float lo, float hi) {
  unsigned r;
  asm("v_cvt_pk_bf16_f32 %0, %1, %2" : "=v"(r) : "v"(lo), "v"(hi));
  return r;
}
__device__ __forceinline__ float bf_lo(unsigned w) { return __uint_as_float(w << 16); }
__device__ __forceinline__ float bf_hi(unsigned w) { return __uint_as_float(w & 0xffff0000u); }
__device__ __forceinline__ float rbf(float x) { return bf_lo(cvtpk(x, x)); }

__device__ __forceinline__ void glds16(const void* base, unsigned off, unsigned lds) {
  unsigned sv;
  asm volatile("s_mov_b32 %0, m0\n\ts_mov_b32 m0, %3\n\ts_nop 0\n\tglobal_load_lds_dwordx4 %1, %2\n\ts_mov_b32 m0, %0"
               : "=&s"(sv) : "v"(off), "s"(base), "s"(lds) : "memory");
}
__device__ __forceinline__ bf16x8 lds_b128(unsigned p) { return *(const LDS_AS bf16x8*)(size_t)p; }
__device__ __forceinline__ s16x4 lds_tr(unsigned p) {
  return __builtin_amdgcn_ds_read_tr16_b64_v4i16((LDS_AS s16x4*)(size_t)p);
}
__device__ __forceinline__ bf16x8 cat4(s16x4 a, s16x4 b) {
  return __builtin_bit_cast(bf16x8, __builtin_shufflevector(a, b, 0, 1, 2, 3, 4, 5, 6, 7));
}
#define MFMA16(a, b, c) __builtin_amdgcn_mfma_f32_16x16x32_bf16(a, b, c, 0, 0, 0)
#define MFMA32(a, b, c) __builtin_amdgcn_mfma_f32_32x32x16_bf16(a, b, c, 0, 0, 0)
#define MFMA8(a, b, c) __builtin_amdgcn_mfma_f32_32x32x8bf16_1k(a, b, c, 0, 0, 0)
#define ZERO16 f32x16{0.f, 0.f, 0.f, 0.f, 0.f, 0.f, 0.f, 0.f, 0.f, 0.f, 0.f, 0.f, 0.f, 0.f, 0.f, 0.f}

constexpr int P_SLOT = 41984;
constexpr int P_SW = 16384, P_SK = 36864;
constexpr int P_R = 3 * P_SLOT;
constexpr int P_GW = P_R + 24576;
constexpr int P_WO = P_GW + 2048;
constexpr int P_BYTES = P_WO + 5120 + 256;

__device__ __forceinline__ void prep_load(const char* x, const char* wq, const char* wk, const char* wv, char* ws,
                                          int qt, int hg, int j, int l, unsigned lds0) {
  STAMP_DECL; STAMP(0);
  __builtin_amdgcn_s_setprio(3);
  unsigned xoff[4], woff[5];
#pragma unroll
  for (int i = 0; i < 4; ++i) {
    const int r = 4 * i + (l >> 4);
    xoff[i] = (16 * j + r) * 1280 + 16 * ((l & 15) ^ r);
  }
#pragma unroll
  for (int i = 0; i < 5; ++i) {
    const int n = 64 * i + l;
    const int pr = n / 10, cc = n - pr * 10;
    const int k = 32 * j + (pr & 24) + ((pr & 7) >> 1) + 4 * (pr & 1);
    woff[i] = k * 1280 + 16 * cc;
  }
  const char* xbase = x + (size_t)qt * 81920;
  const char* wbase = wq + hg * 160;
  const char* kbase = (hg < 2 ? wk : wv);
#define P_ISSUE(c, slot)                                                                       \
  {                                                                                            \
    const unsigned d_ = lds0 + (slot) * P_SLOT;                                                \
    if (hg < 4) {                                                                              \
      glds16(kbase + (c) * 5120, j * 1024 + l * 16, d_ + P_SK + j * 1024);                     \
      if (j == 0) glds16(kbase + (c) * 5120, 4096 + l * 16, d_ + P_SK + 4096);                 \
    }                                                                                          \
    _Pragma("unroll") for (int i = 0; i < 4; ++i) glds16(xbase + (c) * 256, xoff[i], d_ + (4 * j + i) * 1024); \
    const char* b_ = wbase + (size_t)(c) * 128 * 1280;                                         \
    _Pragma("unroll") for (int i = 0; i < 5; ++i) glds16(b_, woff[i], d_ + P_SW + (5 * j + i) * 1024); \
  }
  asm volatile("s_barrier" ::: "memory");
  P_ISSUE(0, 0);
  P_ISSUE(1, 1);
  STAMP(1);
  WAITVM_BAR(9);
  STAMP(2);
  P_ISSUE(2, 2);
  TWAIT(WAITVM_BAR(9));
  STAMP(3);
  P_ISSUE(3, 0);
  TWAIT(WAITVM_BAR(9));
  P_ISSUE(4, 1);
  TWAIT(WAITVM_BAR(9));
  TWAIT(WAITVM_BAR(0));
  STAMP(4);
  STAMP_FLUSH(0);
}

__device__ __forceinline__ void prep_gemm(char* ws, int qt, int hg, int w, int l, unsigned lds0) {
  STAMP_DECL; STAMP(0);
  asm volatile("s_barrier" ::: "memory");
  const int hd = w & 3, qh = w >> 2, h = l >> 5;
  const int row = 32 * qh + (l & 31);
  const unsigned xa = row * 256 + 16 * (h ^ (row & 15));
  const unsigned wa = lds0 + P_SW + (8 * h + 2 * ((l & 15) >> 2)) * 160 + 40 * hd + 32 * ((l >> 4) & 1) + 8 * (l & 3);
  f32x16 acc = ZERO16;
#define Q_CHUNK(slot)                                                                                   \
  _Pragma("unroll") for (int ks = 0; ks < 8; ++ks) {                                                    \
    const bf16x8 xf = lds_b128(lds0 + (xa ^ (32 * ks)) + (slot) * P_SLOT);                              \
    const bf16x8 wf = cat4(lds_tr(wa + (slot) * P_SLOT + ks * 2560), lds_tr(wa + (slot) * P_SLOT + ks * 2560 + 160)); \
    acc = MFMA32(wf, xf, acc);                                                                          \
  }
  STAMP(1);
  BAR_ONLY();
  STAMP(2);
  Q_CHUNK(0);
  TWAIT(BAR_ONLY());
  STAMP(3);
  Q_CHUNK(1);
  TWAIT(BAR_ONLY());
  Q_CHUNK(2);
  TWAIT(BAR_ONLY());
  Q_CHUNK(0);
  TWAIT(BAR_ONLY());
  STAMP(4);
  Q_CHUNK(1);
  STAMP(5);
  char* qf = ws + WS_QF + (size_t)((qt * 8 + hg) * 8 + hd * 2 + qh) * 1536;
  *(u32x4*)(qf + l * 16) = u32x4{cvtpk(acc[0] * QSCALE, acc[1] * QSCALE), cvtpk(acc[2] * QSCALE, acc[3] * QSCALE),
                                 cvtpk(acc[4] * QSCALE, acc[5] * QSCALE), cvtpk(acc[6] * QSCALE, acc[7] * QSCALE)};
  *(u32x2*)(qf + 1024 + l * 8) = u32x2{cvtpk(acc[8] * QSCALE, acc[9] * QSCALE), cvtpk(acc[10] * QSCALE, acc[11] * QSCALE)};
  STAMP(6);
  STAMP_FLUSH(0);
}

__device__ __forceinline__ void prep_help(const char* __restrict__ kin, const char* __restrict__ vin,
                                          const char* __restrict__ shk, const char* wo, const char* gw, char* ws,
                                          int qt, int hg, int j, int l, unsigned lds0) {
  STAMP_DECL; STAMP(0);
  __builtin_amdgcn_s_setprio(3);
  const int s0 = qt * 64, sw = s0 + 16 * j;
  const unsigned rg = lds0 + P_R + j * 6144;
  const int dp = l < 40 ? l : 39;
  const u32x4 sk = *(const u32x4*)(shk + hg * 640 + dp * 16);
#pragma unroll
  for (int i = 0; i < 3; ++i) {
    int n = 64 * i + l;
    n = n < 190 ? n : 189;
    const int rr = n / 10, cc = n - rr * 10;
    const int row = sw + rr - 3;
    glds16(kin, (row < 0 ? 0 : row) * 1280 + hg * 160 + cc * 16, rg + i * 1024);
  }
#pragma unroll
  for (int i = 0; i < 3; ++i) {
    int n = 64 * i + l;
    n = n < 160 ? n : 159;
    const int rr = n / 10, cc = n - rr * 10;
    glds16(vin, (sw + rr) * 1280 + hg * 160 + cc * 16, rg + 3072 + i * 1024);
  }
  if (j == 0) glds16(gw, l * 16, lds0 + P_GW);
  if (j == 1 && l < 16) glds16(gw, 1024 + l * 16, lds0 + P_GW + 1024);
  if (qt < 20) {
#pragma unroll
    for (int i = 0; i < 2; ++i) {
      const int p = (j ^ 3) + 4 * i;
      const int n = p * 64 + l;
      const int pr = n / 10, cc = n - pr * 10;
      const int k = pr ^ (((pr >> 3) & 1) << 2);
      if (p < 5) glds16(wo + hg * 160, (32 * qt + k) * 1280 + 16 * cc, lds0 + P_WO + p * 1024);
    }
  }
  asm volatile("s_barrier" ::: "memory");
  WAIT_VM(0);
  STAMP(1);
  unsigned kk[19], vv[16];
#pragma unroll
  for (int r = 0; r < 19; ++r) kk[r] = *(const LDS_AS unsigned*)(size_t)(rg + dp * 4 + r * 160);
#pragma unroll
  for (int r = 0; r < 16; ++r) vv[r] = *(const LDS_AS unsigned*)(size_t)(rg + 3072 + dp * 4 + r * 160);
  if (sw == 0) kk[0] = kk[1] = kk[2] = 0u;
  typedef __attribute__((ext_vector_type(2))) float f32x2;
  const f32x2 t0 = {bf_lo(sk[0]), bf_lo(sk[2])}, t1 = {bf_hi(sk[0]), bf_hi(sk[2])};
  const f32x2 t2 = {bf_lo(sk[1]), bf_lo(sk[3])}, t3 = {bf_hi(sk[1]), bf_hi(sk[3])};
#define UNP(x) f32x2{bf_lo(x), bf_hi(x)}
  const unsigned stg = rg + dp * 80;
#define U_OCT(r0, o)                                                                                          \
  {                                                                                                           \
    f32x2 u[8];                                                                                               \
    _Pragma("unroll") for (int r = 0; r < 8; ++r)                                                             \
        u[r] = (UNP(kk[r0 + r + 3]) * t0 + UNP(kk[r0 + r + 2]) * t1 + UNP(kk[r0 + r + 1]) * t2 +              \
                UNP(kk[r0 + r]) * t3) * UNP(vv[r0 + r]);                                                      \
    if (l < 40) {                                                                                             \
      _Pragma("unroll") for (int e = 0; e < 2; ++e)                                                           \
        *(LDS_AS u32x4*)(size_t)(stg + 32 * e + (o)) = u32x4{cvtpk(u[7][e], u[6][e]), cvtpk(u[5][e], u[4][e]), \
                                                             cvtpk(u[3][e], u[2][e]), cvtpk(u[1][e], u[0][e])}; \
    }                                                                                                         \
  }
  asm volatile("s_waitcnt lgkmcnt(0)" ::: "memory");
  U_OCT(0, 16);
  const int i16 = l & 15, g = l >> 4, q4 = i16 >> 2, p4 = l & 3;
  const int t = hg & 1, a = j & 1, nt = j >> 1;
  const unsigned xa = (32 * t + 16 * a + i16) * 256 + 16 * (g ^ i16);
  const unsigned wa = lds0 + P_SK + (8 * g + q4) * 40 + 32 * nt + 8 * p4;
  const int grow = 8 * hg + 2 * j + (l >> 5), gi = l & 31;
  const unsigned gxa = lds0 + grow * 256 + 16 * ((gi >> 1) ^ (grow & 15)) + 8 * (gi & 1);
  f32x4 acc = {0.f, 0.f, 0.f, 0.f};
  float gacc = 0.f;
#define KVG(c, slot)                                                                                    \
  {                                                                                                     \
    const u32x2 xv = *(const LDS_AS u32x2*)(size_t)(gxa + (slot) * P_SLOT);                             \
    const u32x2 wv = *(const LDS_AS u32x2*)(size_t)(lds0 + P_GW + gi * 8 + (c) * 256);                  \
    if (hg < 4) {                                                                                       \
      bf16x8 wf[4], xf[4];                                                                              \
      _Pragma("unroll") for (int ks = 0; ks < 4; ++ks) {                                                \
        wf[ks] = cat4(lds_tr(wa + (slot) * P_SLOT + ks * 1280), lds_tr(wa + (slot) * P_SLOT + ks * 1280 + 160)); \
        xf[ks] = lds_b128(lds0 + (slot) * P_SLOT + (xa ^ (64 * ks)));                                   \
      }                                                                                                 \
      if (hg < 2) { _Pragma("unroll") for (int ks = 0; ks < 4; ++ks) acc = MFMA16(wf[ks], xf[ks], acc); } \
      else { _Pragma("unroll") for (int ks = 0; ks < 4; ++ks) acc = MFMA16(xf[ks], wf[ks], acc); }      \
    }                                                                                                   \
    const unsigned p0 = cvtpk(bf_lo(xv[0]) * bf_lo(wv[0]), bf_hi(xv[0]) * bf_hi(wv[0]));                \
    const unsigned p1 = cvtpk(bf_lo(xv[1]) * bf_lo(wv[1]), bf_hi(xv[1]) * bf_hi(wv[1]));                \
    gacc += (bf_lo(p0) + bf_hi(p0)) + (bf_lo(p1) + bf_hi(p1));                                          \
  }
  STAMP(2);
  BAR_ONLY();
  KVG(0, 0);
  U_OCT(8, 0);
  TWAIT(BAR_ONLY());
  STAMP(3);
  KVG(1, 1);
  {
#pragma unroll
    for (int i = 0; i < 3; ++i) {
      const int r = 20 * j + 8 * i + (l >> 3), pc = l & 7;
      if (i < 2 || l < 32) {
        const u32x4 v = *(const LDS_AS u32x4*)(size_t)(lds0 + P_R + (3 - (pc >> 1)) * 6144 + (r >> 1) * 80 + (r & 1) * 32 + (pc & 1) * 16);
        *(u32x4*)(ws + WS_UT + (size_t)(hg * 80 + r) * 4096 + (1984 - s0) * 2 + pc * 16) = v;
      }
    }
  }
  TWAIT(BAR_ONLY());
  KVG(2, 2);
  if (qt < 20) {
    const unsigned wa0 = lds0 + P_WO + (8 * g + 4 * (g & 1) + q4) * 160 + 8 * p4;
    const unsigned wa1 = lds0 + P_WO + (8 * g + 4 * (1 - (g & 1)) + q4) * 160 + 8 * p4;
    char* wf = ws + WS_WF + (size_t)(hg * 20 + qt) * 5120 + l * 16;
    *(bf16x8*)(wf + j * 1024) = cat4(lds_tr(wa0 + 32 * j), lds_tr(wa1 + 32 * j));
    if (j == 0) *(bf16x8*)(wf + 4096) = cat4(lds_tr(wa0 + 128), lds_tr(wa1 + 128));
  }
  TWAIT(BAR_ONLY());
  KVG(3, 0);
  TWAIT(BAR_ONLY());
  KVG(4, 1);
  STAMP(4);
  if (hg < 4) {
    u32x2 v = {cvtpk(acc[0], acc[1]), cvtpk(acc[2], acc[3])};
    char* kv = ws + WS_KV + qt * 7168;
    if (hg < 2) {
      const int key = 16 * a + i16;
      if (nt == 0) {
        *(u32x2*)(kv + 1024 * t + 16 * (key + 32 * (g & 1)) + 8 * (g >> 1)) = v;
      } else if (g < 2) {
        if (g == 1) v = u32x2{0x3f803f80u, 0x00003f80u};
        *(u32x2*)(kv + 2048 + 16 * (key + 32 * g) + 8 * t) = v;
      }
    } else {
      const int d = 16 * nt + i16;
      if (d >= 20) {
        const unsigned c = (d == 20 || d == 24) ? 0x3f803f80u : 0u;
        v = u32x2{c, c};
      }
      *(u32x2*)(kv + 3072 + 2048 * t + 1024 * a + 16 * (d + 32 * (g >> 1)) + 8 * (g & 1)) = v;
    }
  }
  gacc += __int_as_float(__builtin_amdgcn_update_dpp(0, __float_as_int(gacc), 0x111, 0xf, 0xf, true));
  gacc += __int_as_float(__builtin_amdgcn_update_dpp(0, __float_as_int(gacc), 0x112, 0xf, 0xf, true));
  gacc += __int_as_float(__builtin_amdgcn_update_dpp(0, __float_as_int(gacc), 0x114, 0xf, 0xf, true));
  gacc += __int_as_float(__builtin_amdgcn_update_dpp(0, __float_as_int(gacc), 0x118, 0xf, 0xf, true));
  gacc += __int_as_float(__builtin_amdgcn_update_dpp(0, __float_as_int(gacc), 0x142, 0xa, 0xf, true));
  const float sg = rbf(__builtin_amdgcn_rcpf(1.0f + __expf(-rbf(gacc))));
  if (gi == 31) ((float*)(ws + WS_G))[s0 + grow] = sg;
  STAMP(5);
  STAMP_FLUSH(0);
}

__global__ void __launch_bounds__(1024) __attribute__((amdgpu_waves_per_eu(4, 4)))
k_prep(const char* __restrict__ x, const char* __restrict__ kin, const char* __restrict__ vin,
       const char* __restrict__ shk, const char* __restrict__ wq, const char* __restrict__ wk,
       const char* __restrict__ wv, const char* __restrict__ wo, const char* __restrict__ gw,
       char* __restrict__ ws) {
  extern __shared__ __attribute__((aligned(16))) char smem[];
  const unsigned lds0 = (unsigned)(size_t)(lds_ptr)smem;
  const int bid = blockIdx.x;
  const int qt = (bid & 7) * 4 + ((bid >> 3) & 3), hg = bid >> 5;
  const int l = threadIdx.x & 63;
  const int w = __builtin_amdgcn_readfirstlane(threadIdx.x >> 6);
  PB(0);
  if (w < 4) prep_help(kin, vin, shk, wo, gw, ws, qt, hg, w, l, lds0);
  else if (w < 8) prep_load(x, wq, wk, wv, ws, qt, hg, w - 4, l, lds0);
  else prep_gemm(ws, qt, hg, w - 8, l, lds0);
  PE(0);
}

constexpr int CHUNK = 14336;
constexpr int M_RING = 0;
constexpr int CV_H = 4160, CV_U = 4640, CV_CH = 4 * CV_H + 2 * CV_U;
constexpr int M_CV = 4 * CHUNK;
constexpr int M_BYTES = M_CV + 3 * CV_CH;

__device__ __forceinline__ f32x16 pv_lo(bf16x8 v, f32x16 p, f32x16 o, int idx) {
  const f32x8 h = __builtin_shufflevector(p, p, 0, 1, 2, 3, 4, 5, 6, 7);
  return __builtin_amdgcn_smfmac_f32_32x32x32_bf16(v, __builtin_bit_cast(bf16x16, h), o, idx, 0, 0);
}
__device__ __forceinline__ f32x16 pv_hi(bf16x8 v, f32x16 p, f32x16 o, int idx) {
  const f32x8 h = __builtin_shufflevector(p, p, 8, 9, 10, 11, 12, 13, 14, 15);
  return __builtin_amdgcn_smfmac_f32_32x32x32_bf16(v, __builtin_bit_cast(bf16x16, h), o, idx, 0, 0);
}
#define EX2(v) __builtin_amdgcn_exp2f(v)
__device__ __forceinline__ s16x4 lo4(bf16x8 v) {
  return __builtin_bit_cast(s16x4, __builtin_shufflevector(v, v, 0, 1, 2, 3));
}
__device__ __forceinline__ s16x4 hi4(bf16x8 v) {
  return __builtin_bit_cast(s16x4, __builtin_shufflevector(v, v, 4, 5, 6, 7));
}
__device__ __forceinline__ float xhalf_max(float m) {
  auto r = __builtin_amdgcn_permlane32_swap(__float_as_uint(m), __float_as_uint(m), false, false);
  return fmaxf(__uint_as_float(r[0]), __uint_as_float(r[1]));
}
__device__ __forceinline__ float max16(const f32x16& s) {
  float a = fmaxf(fmaxf(s[0], s[1]), s[2]), b = fmaxf(fmaxf(s[3], s[4]), s[5]);
  a = fmaxf(fmaxf(a, s[6]), s[7]); b = fmaxf(fmaxf(b, s[8]), s[9]);
  a = fmaxf(fmaxf(a, s[10]), s[11]); b = fmaxf(fmaxf(b, s[12]), s[13]);
  a = fmaxf(fmaxf(a, s[14]), s[15]);
  return fmaxf(a, b);
}

__device__ __forceinline__ void attn_wave(char* ws, int qt, int hg, int w, int l, unsigned lds0) {
  STAMP_DECL; STAMP(0);
  const char* kvg = ws + WS_KV;
  const int h = l >> 5, hd = w & 3, kh = w >> 2;
  if (w < 7) {
    glds16(kvg, (2 * w >= 7 ? 15 * 7168 : 0) + 2 * w * 1024 + l * 16, lds0 + M_RING + w * 2048);
    glds16(kvg, (2 * w + 1 >= 7 ? 15 * 7168 : 0) + (2 * w + 1) * 1024 + l * 16, lds0 + M_RING + w * 2048 + 1024);
  }
  const char* qf = ws + WS_QF + (size_t)((qt * 8 + hg) * 8 + hd * 2) * 1536;
  const bf16x8 qa1 = *(const bf16x8*)(qf + kh * 1536 + l * 16);
  const bf16x8 qa2 = *(const bf16x8*)(qf + (kh ^ 1) * 1536 + l * 16);
  u32x2 q81 = *(const u32x2*)(qf + kh * 1536 + 1024 + l * 8);
  u32x2 q82 = *(const u32x2*)(qf + (kh ^ 1) * 1536 + 1024 + l * 8);
  unsigned oo0, oo1, oo2;
  {
    const int q = l & 31;
    const int col = (q & 8) | ((q & 1) << 2) | ((q & 7) >> 1);
    const unsigned ob = (2 * kh + (q >> 4)) * 1024 + col * 16;
    const int k0 = (hg * 4 + hd) * 20 + 4 * h;
#define O_OFF(k) (ob + ((k) >> 5) * 4096 + (((k) >> 3) & 3) * 256 + ((k) & 7) * 2)
    oo0 = O_OFF(k0); oo1 = O_OFF(k0 + 8); oo2 = O_OFF(k0 + 16);
#undef O_OFF
  }
  const int idx = 0xdddd;
  const f32x16 zero = ZERO16;
  f32x16 O1 = zero, O2 = zero, SA, SB, SC, SD = zero;
  PIN(O1); PIN(O2); PIN(SD); PIN(oo0); PIN(oo1); PIN(oo2);
  if (h == 1) {
    q81 = u32x2{0u, 0u};
    q82 = u32x2{0u, 0u};
  }
  PIN(q81); PIN(q82);
  STAMP(1);
  WAITVM_BAR(0);
  STAMP(2);
  {
    const bf16x8 k0 = lds_b128(lds0 + M_RING + l * 16);
    const s16x4 e0 = lo4(lds_b128(lds0 + M_RING + 2048 + l * 16));
    SA = MFMA8(e0, __builtin_bit_cast(s16x4, q81), MFMA32(k0, qa1, zero));
    SB = MFMA8(e0, __builtin_bit_cast(s16x4, q82), MFMA32(k0, qa2, zero));
    const float m1 = xhalf_max(max16(SA)), m2 = xhalf_max(max16(SB));
    if (h == 1) {
      q81[0] = cvtpk(-m1, 0.f);
      q82[0] = cvtpk(-m2, 0.f);
    }
  }
  const s16x4 qb1 = __builtin_bit_cast(s16x4, q81), qb2 = __builtin_bit_cast(s16x4, q82);
  const unsigned ra = lds0 + M_RING + kh * 7168 + l * 16;
#define LDF(c, off) lds_b128(ra + (c) + (off))
  bf16x8 k16 = LDF(0, 0), k8p = LDF(0, 2048), v0 = LDF(0, 3072), v1 = v0;
  SA = MFMA8(lo4(k8p), qb1, MFMA32(k16, qa1, zero));
  SB = MFMA8(lo4(k8p), qb2, MFMA32(k16, qa2, zero));
  k16 = LDF(0, 1024);
#define E4(X, i)                                                                                   \
  X[i] = EX2(X[i]); X[i + 1] = EX2(X[i + 1]); X[i + 2] = EX2(X[i + 2]); X[i + 3] = EX2(X[i + 3]); \
  PIN(X); SBAR();
#define E2(X, i) X[i] = EX2(X[i]); X[i + 1] = EX2(X[i + 1]); PIN(X); SBAR();
#define PRIO(n) __builtin_amdgcn_s_setprio(n); SBAR();
#define HALF1(C1, N1, N2, K8, LV1)                                               \
  {                                                                              \
    E2(C1, 0); O2 = pv_hi(v1, N2, O2, idx); v1 = LV1; SBAR();                    \
    E4(C1, 2); N1 = MFMA32(k16, qa1, zero); SBAR();                              \
    E4(C1, 6); N1 = MFMA8(K8, qb1, N1); SBAR();                                  \
    E4(C1, 10); O1 = pv_lo(v0, C1, O1, idx); SBAR();                             \
    E2(C1, 14);                                                                  \
  }
#define HALF2(C1, C2, N2, K8, LK16, LK8, LV0)                                    \
  {                                                                              \
    E2(C2, 0); O1 = pv_hi(v1, C1, O1, idx); SBAR();                              \
    E4(C2, 2); N2 = MFMA32(k16, qa2, zero); k16 = LK16; SBAR();                  \
    E4(C2, 6); N2 = MFMA8(K8, qb2, N2); LK8; SBAR();                             \
    E4(C2, 10); O2 = pv_lo(v0, C2, O2, idx); v0 = LV0; SBAR();                   \
    E2(C2, 14);                                                                  \
  }
  STAMP(5);
  for (int c = 0; c < 16; ++c) {
    const unsigned cur = (c & 3) * CHUNK, nxt = ((c + 1) & 3) * CHUNK;
    HALF1(SA, SC, SD, hi4(k8p), LDF(cur, 4096));
    asm volatile("s_barrier" ::: "memory");
    PRIO(3);
    HALF2(SA, SB, SD, hi4(k8p), LDF(nxt, 0), k8p = LDF(nxt, 2048), LDF(cur, 5120));
    PRIO(2);
    HALF1(SC, SA, SB, lo4(k8p), LDF(cur, 6144));
    PRIO(1);
    HALF2(SC, SD, SB, lo4(k8p), LDF(nxt, 1024), (void)0, LDF(nxt, 3072));
    PRIO(0);
  }
  O2 = pv_hi(v1, SD, O2, idx);
  STAMP(3);
  {
    const unsigned sa = lds0 + CHUNK + w * 3328 + l * 16;
#pragma unroll
    for (int i = 0; i < 3; ++i)
      *(LDS_AS f32x4*)(size_t)(sa + i * 1024) = f32x4{O2[4 * i], O2[4 * i + 1], O2[4 * i + 2], O2[4 * i + 3]};
    *(LDS_AS float*)(size_t)(lds0 + CHUNK + w * 3328 + 3072 + l * 4) = O2[12];
  }
  BAR_ONLY();
  f32x16 O = O1;
  {
    const unsigned sa = lds0 + CHUNK + (w ^ 4) * 3328 + l * 16;
#pragma unroll
    for (int i = 0; i < 3; ++i) {
      const f32x4 t = *(const LDS_AS f32x4*)(size_t)(sa + i * 1024);
      O[4 * i] += t[0]; O[4 * i + 1] += t[1]; O[4 * i + 2] += t[2]; O[4 * i + 3] += t[3];
    }
    O[12] += *(const LDS_AS float*)(size_t)(lds0 + CHUNK + (w ^ 4) * 3328 + 3072 + l * 4);
  }
  float lsum = h ? O[8] : O[12];
  typedef __attribute__((ext_vector_type(2))) float f32x2;
  f32x2 nz = f32x2{lsum, O[0]} * 0.f;
#pragma unroll
  for (int i = 1; i < 11; i += 2) nz += f32x2{O[i], O[i + 1]} * 0.f;
  const bool ok = (lsum >= 9.094947e-13f) && (lsum <= 1.0e36f) && (fmaf(O[11], 0.f, nz[0] + nz[1]) == 0.f);
  if (__builtin_expect(__any(!ok), 0)) {
    const bf16x8 qa = qa1;
    u32x2 q8 = q81;
    if (h == 1) q8 = u32x2{0u, 0u};
    const char* ga = kvg + l * 16;
    float m = -INFINITY;
    for (int t = 0; t < 64; ++t) {
      const char* b = ga + (t >> 1) * 7168;
      const f32x16 s = MFMA8(*(const s16x4*)(b + 2048 + 8 * (t & 1)), __builtin_bit_cast(s16x4, q8),
                             MFMA32(*(const bf16x8*)(b + 1024 * (t & 1)), qa, zero));
      m = fmaxf(m, max16(s));
    }
    m = xhalf_max(m);
    const float m0 = rbf(m), m1 = rbf(m - m0), m2 = rbf(m - m0 - m1);
    if (h == 1) q8 = u32x2{cvtpk(-m0, -m1), cvtpk(-m2, 0.f)};
    O = zero;
    for (int t = 0; t < 64; ++t) {
      const char* b = ga + (t >> 1) * 7168;
      f32x16 s = MFMA8(*(const s16x4*)(b + 2048 + 8 * (t & 1)), __builtin_bit_cast(s16x4, q8),
                       MFMA32(*(const bf16x8*)(b + 1024 * (t & 1)), qa, zero));
#pragma unroll
      for (int i = 0; i < 16; ++i) s[i] = EX2(s[i]);
      O = pv_lo(*(const bf16x8*)(b + 3072 + 2048 * (t & 1)), s, O, idx);
      O = pv_hi(*(const bf16x8*)(b + 4096 + 2048 * (t & 1)), s, O, idx);
    }
    lsum = h ? O[8] : O[12];
  }
  const float inv = __builtin_amdgcn_rcpf(lsum);
  char* ob = ws + WS_OF + (size_t)qt * 81920;
  *(u32x2*)(ob + oo0) = u32x2{cvtpk(O[0] * inv, O[1] * inv), cvtpk(O[2] * inv, O[3] * inv)};
  *(u32x2*)(ob + oo1) = u32x2{cvtpk(O[4] * inv, O[5] * inv), cvtpk(O[6] * inv, O[7] * inv)};
  if (h == 0) *(u32x2*)(ob + oo2) = u32x2{cvtpk(O[8] * inv, O[9] * inv), cvtpk(O[10] * inv, O[11] * inv)};
  STAMP(4);
  STAMP_FLUSH(1);
}

#define CONV_PER 2
#define CONV_PRIO 3
#define CONV_GAP() SBAR()
struct Ring {
  const char* src;
  unsigned off, dst;
  int next;
};
template <int J>
__device__ __forceinline__ void ring_issue(Ring& r) {
  const char* b = r.src + r.next * 7168;
  const unsigned d = r.dst + (r.next & 3) * CHUNK;
  glds16(b, r.off, d);
  glds16(b, r.off + (J + 4 >= 7 ? 15 * 7168 : 0) + 4096, d + 4096);
  glds16(b, r.off + 15 * 7168 + 8192, d + 8192);
  if (J < 2) glds16(b, r.off + 15 * 7168 + 12288, d + 12288);
  ++r.next;
}
template <int J>
__device__ __forceinline__ void ring_step(Ring& r) {
  if (r.next < 16) {
    if (J < 2) WAIT_BAR(4); else WAIT_BAR(3);
    ring_issue<J>(r);
  } else {
    WAIT_BAR(0);
    ++r.next;
  }
}

template <int J>
__device__ __forceinline__ void conv_wave(const char* __restrict__ hk, const char* __restrict__ sd, const char* xin, char* ws, int bid,
                                          int l, unsigned lds0) {
  STAMP_DECL; STAMP(0);
  __builtin_amdgcn_s_setprio(CONV_PRIO);
  Ring rg = {ws + WS_KV, (unsigned)(J * 1024 + l * 16), lds0 + M_RING + J * 1024, 1};
  ring_issue<J>(rg);
  ring_issue<J>(rg);
  asm volatile("s_barrier" ::: "memory");
  STAMP(1);
  const int nch = bid < 128 ? 3 : 2;
  const int ch0 = bid < 128 ? 3 * bid : 384 + 2 * (bid - 128);
  const unsigned cv0 = lds0 + M_CV;
  unsigned hv[3], dv[3], pf0, pf1;
#pragma unroll
  for (int c = 0; c < 3; ++c) {
    const int chc = ch0 + (c < nch ? c : 0);
    const char* hb = hk + (size_t)chc * 4096;
    const int e0 = 2 * (l & 15) - 30 + 2 * J;
    asm volatile("global_load_dword %0, %2, %3\n\tglobal_load_ushort %1, %4, %5"
                 : "=&v"(hv[c]), "=&v"(dv[c])
                 : "v"((e0 < 0 ? 0 : e0) * 2), "s"(hb), "v"(chc * 2), "s"(sd) : "memory");
    if (c < nch) {
      const char* ub = ws + WS_UT + (size_t)chc * 4096;
#pragma unroll
      for (int i = 0; i < 4; ++i) {
        int e = 2 + 2 * J + 512 * i + 8 * l;
        e = e > 2040 ? 2040 : e;
        glds16(hb, e * 2, cv0 + c * CV_CH + J * CV_H + 64 + i * 1024);
      }
#pragma unroll
      for (int i = 0; i < 2; ++i)
        glds16(ub, (J < 2 ? 2 : 0) + (J & 1) * 2048 + i * 1024 + l * 16,
               cv0 + c * CV_CH + 4 * CV_H + (J >> 1) * CV_U + (J & 1) * 2048 + i * 1024);
      if (J < 2) WAIT_BAR(12); else WAIT_BAR(11);
    } else {
      if (J < 2) WAIT_BAR(6); else WAIT_BAR(5);
    }
    ring_issue<J>(rg);
  }
  ring_step<J>(rg);
  asm volatile("" : "+v"(hv[0]), "+v"(hv[1]), "+v"(hv[2]), "+v"(dv[0]), "+v"(dv[1]), "+v"(dv[2]));
#pragma unroll
  for (int c = 0; c < 3; ++c) {
    const int e0 = 2 * (l & 15) - 30 + 2 * J;
    if (l < 16) *(LDS_AS unsigned*)(size_t)(cv0 + c * CV_CH + J * CV_H + l * 4) = e0 < 0 ? 0u : hv[c];
    if (J < 2 && l < 28) *(LDS_AS u32x4*)(size_t)(cv0 + c * CV_CH + 4 * CV_H + J * CV_U + 4096 + l * 16) = u32x4{0, 0, 0, 0};
    if (J == 1 && l == 63) *(LDS_AS u16*)(size_t)(cv0 + c * CV_CH + 4 * CV_H + 4094) = 0;
  }
  ring_step<J>(rg);
  {
    const int qt = (bid & 7) * 4 + ((bid >> 3) & 3), hg = bid >> 5;
    unsigned d0, d1;
    const int n = J * 64 + l;
    asm volatile("global_load_dword %0, %2, %3\n\tglobal_load_dword %1, %4, %5"
                 : "=&v"(d0), "=&v"(d1)
                 : "v"(hg * 102400 + (qt & 3) * 25600 + (n < 200 ? n : 199) * 128), "s"(ws + WS_WF),
                   "v"((qt * 64 + (n >> 2)) * 1280 + hg * 160 + (n & 3) * 52), "s"(xin) : "memory");
    pf0 = d0;
    pf1 = d1;
  }
  STAMP(2);
  const int i16 = l & 15, kq = l >> 4;
  const unsigned aa = cv0 + (i16 & 3) * CV_H + 16 * ((i16 >> 2) + kq);
  const unsigned ba = cv0 + 4 * CV_H + (i16 & 1) * CV_U + 2 * (2016 - 32 * (i16 >> 1) + 8 * kq) - 512 * (7 - J);
  const int jj0 = (30 - 2 * (i16 & 3) - 8 * (i16 >> 2) - 8 * kq);
  int cnt = 0;
#pragma unroll 1
  for (int c = 0; c < nch; ++c) {
    const unsigned db = c == 0 ? dv[0] : (c == 1 ? dv[1] : dv[2]);
    const u32x4 dm = {jj0 == 0 ? db : 0u, jj0 == 2 ? db : 0u, jj0 == 4 ? db : 0u, jj0 == 6 ? db : 0u};
    const bf16x8 dfrag = __builtin_bit_cast(bf16x8, dm);
    unsigned ap = aa + c * CV_CH, bp = ba + c * CV_CH;
    bf16x8 A[8 - J], B[8 - J];
#pragma unroll
    for (int a = 0; a < 8 - J; ++a) {
      A[a] = lds_b128(ap + 512 * a);
      B[a] = lds_b128(bp + 512 * a);
    }
    f32x4 hi = MFMA16(dfrag, B[0], (f32x4{0.f, 0.f, 0.f, 0.f}));
    f32x4 lo = MFMA16(dfrag, B[7 - 2 * J], (f32x4{0.f, 0.f, 0.f, 0.f}));
#pragma unroll 1
    for (int r = 0; r < 8; ++r) {
      ap += 64;
      bp += 64;
      PIN(ap);
      PIN(bp);
#pragma unroll
      for (int a = 0; a < 8 - J; ++a) {
        CONV_GAP();
        hi = MFMA16(A[a], B[a], hi);
        if (a <= J) {
          PIN(hi);
          CONV_GAP();
          lo = MFMA16(A[a], B[a + 7 - 2 * J], lo);
          PIN(lo);
        } else {
          PIN(hi);
        }
        A[a] = lds_b128(ap + 512 * a);
        B[a] = lds_b128(bp + 512 * a);
        SBAR();
      }
      if (++cnt == CONV_PER) {
        cnt = (rg.next & 1) ? -1 : 0;
        ring_step<J>(rg);
      }
    }
    char* zb = ws + WS_ZT + (size_t)(ch0 + c) * 4096 + (32 * (i16 >> 1) + 8 * kq + 4 * (i16 & 1)) * 2;
    *(u32x2*)(zb + 512 * (7 - J)) = u32x2{cvtpk(hi[0], hi[1]), cvtpk(hi[2], hi[3])};
    *(u32x2*)(zb + 512 * J) = u32x2{cvtpk(lo[0], lo[1]), cvtpk(lo[2], lo[3])};
  }
  STAMP(3);
  while (rg.next < 18) ring_step<J>(rg);
  asm volatile("" :: "v"(pf0), "v"(pf1));
  STAMP(4);
  STAMP_FLUSH(1);
}

__global__ void __launch_bounds__(768) __attribute__((amdgpu_waves_per_eu(3, 3)))
k_main(const char* __restrict__ hk, const char* __restrict__ sd, const char* __restrict__ xin, char* __restrict__ ws) {
  extern __shared__ __attribute__((aligned(16))) char smem[];
  const unsigned lds0 = (unsigned)(size_t)(lds_ptr)smem;
  const int bid = blockIdx.x;
  const int qt = (bid & 7) * 4 + ((bid >> 3) & 3), hg = bid >> 5;
  const int l = threadIdx.x & 63;
  const int w = __builtin_amdgcn_readfirstlane(threadIdx.x >> 6);
  PB(1);
  if (w < 8) attn_wave(ws, qt, hg, w, l, lds0);
  else if (w == 8) conv_wave<0>(hk, sd, xin, ws, bid, l, lds0);
  else if (w == 9) conv_wave<1>(hk, sd, xin, ws, bid, l, lds0);
  else if (w == 10) conv_wave<2>(hk, sd, xin, ws, bid, l, lds0);
  else conv_wave<3>(hk, sd, xin, ws, bid, l, lds0);
  PE(1);
}

constexpr int O_W = 0;
constexpr int O_Z = 102400;
constexpr int O_X = O_Z + 10240;
constexpr int O_RED = O_X + 10240;
constexpr int O_BYTES = O_RED + 24576;

template <int KH>
__device__ __forceinline__ void out_body(const char* __restrict__ x, char* __restrict__ ws, char* __restrict__ out,
                                         int qt, int hg, int w, int l, unsigned lds0) {
  STAMP_DECL; STAMP(0);
  const int s0 = qt * 64, st = w & 3;
  const char* of = ws + WS_OF + (size_t)qt * 81920 + KH * 10 * 4096 + st * 1024;
  const char* wf = ws + WS_WF + (size_t)hg * 102400 + KH * 51200;
  const unsigned wd = lds0 + O_W + KH * 51200;
  bf16x8 o_[10];
#define O_LD(i) asm volatile("global_load_dwordx4 %0, %1, %2" : "=&v"(o_[i]) : "v"(l * 16 + (i) * 4096), "s"(of) : "memory")
#define W_LD(p) glds16(wf, (p) * 1024 + l * 16, wd + (p) * 1024)
  O_LD(0); O_LD(1); O_LD(2); O_LD(3);
#pragma unroll
  for (int i = 0; i < 5; ++i) W_LD(st + 4 * i);
  const int i16 = l & 15, g = l >> 4;
  constexpr int NT = KH ? 2 : 3, T0 = KH ? 3 : 0;
  const int srel = 16 * st + ((i16 & 8) | ((i16 & 3) << 1) | ((i16 >> 2) & 1));
  {
    const int n = w * 80 + l, n2 = w * 80 + 64 + (l & 15);
    glds16(ws + WS_ZT, (hg * 80 + (n >> 3)) * 4096 + s0 * 2 + (n & 7) * 16, lds0 + O_Z + w * 1280);
    if (l < 16) glds16(ws + WS_ZT, (hg * 80 + (n2 >> 3)) * 4096 + s0 * 2 + (n2 & 7) * 16, lds0 + O_Z + w * 1280 + 1024);
    const int r1 = l / 10, r2 = (64 + (l & 15)) / 10;
    glds16(x, (s0 + 8 * w + r1) * 1280 + hg * 160 + (l - 10 * r1) * 16, lds0 + O_X + w * 1280);
    if (l < 16) glds16(x, (s0 + 8 * w + r2) * 1280 + hg * 160 + (64 + l - 10 * r2) * 16, lds0 + O_X + w * 1280 + 1024);
  }
  float gt;
  asm volatile("global_load_dword %0, %1, %2" : "=&v"(gt) : "v"((s0 + srel) * 4), "s"(ws + WS_G) : "memory");
  O_LD(4); O_LD(5); O_LD(6);
#pragma unroll
  for (int i = 0; i < 4; ++i) { const int p = st + 4 * i; W_LD(20 + (p < 15 ? p : 14)); }
  f32x4 acc[5];
#pragma unroll
  for (int nt = 0; nt < 5; ++nt) acc[nt] = f32x4{0.f, 0.f, 0.f, 0.f};
  unsigned wa = wd + l * 16;
  unsigned za = lds0 + O_Z + (4 * g + (i16 >> 2)) * 128 + 32 * st + 8 * (l & 3) + T0 * 2048;
  unsigned xa = lds0 + O_X + srel * 160 + (16 * T0 + 4 * g) * 2;
  unsigned ra = lds0 + O_RED + l * 16;
  unsigned oa = (s0 + srel) * 1280 + (hg * 80 + 16 * T0 + 4 * g) * 2;
  PIN(wa); PIN(za); PIN(xa); PIN(ra); PIN(oa);
#define O_GRP(k0, n)                                                                               \
  {                                                                                                \
    bf16x8 wf_[(n) * 5];                                                                           \
    _Pragma("unroll") for (int i = 0; i < (n) * 5; ++i) wf_[i] = lds_b128(wa + ((k0) * 5 + i) * 1024); \
    SBAR();                                                                                        \
    _Pragma("unroll") for (int i = 0; i < (n) * 5; ++i) acc[i % 5] = MFMA16(wf_[i], o_[(k0) + i / 5], acc[i % 5]); \
    asm volatile("" : "+v"(acc[0]), "+v"(acc[1]), "+v"(acc[2]), "+v"(acc[3]), "+v"(acc[4]));      \
    SBAR();                                                                                        \
  }
  STAMP(1);
  asm volatile("s_waitcnt vmcnt(12)\n\ts_barrier" : "+v"(o_[0]), "+v"(o_[1]), "+v"(o_[2]), "+v"(o_[3]) :: "memory");
  STAMP(2);
  O_GRP(0, 4);
  O_LD(7); O_LD(8); O_LD(9);
#pragma unroll
  for (int i = 0; i < 4; ++i) { const int p = st + 4 * i; W_LD(35 + (p < 15 ? p : 14)); }
  asm volatile("s_waitcnt vmcnt(7)\n\ts_barrier" : "+v"(o_[4]), "+v"(o_[5]), "+v"(o_[6]), "+v"(gt) :: "memory");
  O_GRP(4, 3);
  u32x2 ga[NT];
#pragma unroll
  for (int t = 0; t < NT; ++t) {
    const u32x2 z = __builtin_bit_cast(u32x2, lds_tr(za + t * 2048));
    const u32x2 xv = *(const LDS_AS u32x2*)(size_t)(xa + t * 32);
    const unsigned a01 = cvtpk(bf_lo(xv[0]) * bf_lo(z[0]), bf_hi(xv[0]) * bf_hi(z[0]));
    const unsigned a23 = cvtpk(bf_lo(xv[1]) * bf_lo(z[1]), bf_hi(xv[1]) * bf_hi(z[1]));
    ga[t] = u32x2{cvtpk(gt * bf_lo(a01), gt * bf_hi(a01)), cvtpk(gt * bf_lo(a23), gt * bf_hi(a23))};
    PIN(ga[t]);
  }
  float og = rbf(1.0f - gt);
  PIN(og);
  asm volatile("s_waitcnt vmcnt(0)\n\ts_barrier" : "+v"(o_[7]), "+v"(o_[8]), "+v"(o_[9]) :: "memory");
  STAMP(3);
  O_GRP(7, 3);
#pragma unroll
  for (int t = 0; t < 5 - NT; ++t) *(LDS_AS f32x4*)(size_t)(ra + w * 3072 + t * 1024) = acc[(KH ? 0 : 3) + t];
  BAR_ONLY();
  STAMP(4);
#pragma unroll
  for (int t = 0; t < NT; ++t) {
    const f32x4 b = acc[T0 + t] + *(const LDS_AS f32x4*)(size_t)(ra + (w ^ 4) * 3072 + t * 1024);
    *(u32x2*)(out + oa + t * 32) = u32x2{cvtpk(fmaf(og, b[0], bf_lo(ga[t][0])), fmaf(og, b[1], bf_hi(ga[t][0]))),
                                         cvtpk(fmaf(og, b[2], bf_lo(ga[t][1])), fmaf(og, b[3], bf_hi(ga[t][1])))};
  }
  STAMP(5);
  STAMP_FLUSH(2);
}

__global__ void __launch_bounds__(512) __attribute__((amdgpu_waves_per_eu(2, 2)))
k_out(const char* __restrict__ x, char* __restrict__ ws, char* __restrict__ out) {
  extern __shared__ __attribute__((aligned(16))) char smem[];
  const unsigned lds0 = (unsigned)(size_t)(lds_ptr)smem;
  const int bid = blockIdx.x;
  const int qt = (bid & 7) * 4 + ((bid >> 3) & 3), hg = bid >> 5;
  const int l = threadIdx.x & 63;
  const int w = __builtin_amdgcn_readfirstlane(threadIdx.x >> 6);
  PB(2);
  if (w < 4) out_body<0>(x, ws, out, qt, hg, w, l, lds0);
  else out_body<1>(x, ws, out, qt, hg, w, l, lds0);
  PE(2);
}

extern "C" void kernel_launch(void* const* d_in, const int* in_sizes, int n_in, void* d_out, int out_size, void* d_ws,
                              size_t ws_size, hipStream_t stream) {
  const char* x = (const char*)d_in[0];
  const char* kin = (const char*)d_in[1];
  const char* vin = (const char*)d_in[2];
  const char* shk = (const char*)d_in[3];
  const char* hk = (const char*)d_in[4];
  const char* sd = (const char*)d_in[5];
  const char* wq = (const char*)d_in[7];
  const char* wk = (const char*)d_in[8];
  const char* wv = (const char*)d_in[9];
  const char* wo = (const char*)d_in[10];
  const char* gw = (const char*)d_in[11];
  char* ws = (char*)d_ws;
  (void)hipFuncSetAttribute((const void*)k_prep, hipFuncAttributeMaxDynamicSharedMemorySize, P_BYTES);
  (void)hipFuncSetAttribute((const void*)k_main, hipFuncAttributeMaxDynamicSharedMemorySize, M_BYTES);
  (void)hipFuncSetAttribute((const void*)k_out, hipFuncAttributeMaxDynamicSharedMemorySize, O_BYTES);
  k_prep<<<256, 1024, P_BYTES, stream>>>(x, kin, vin, shk, wq, wk, wv, wo, gw, ws);
  k_main<<<256, 768, M_BYTES, stream>>>(hk, sd, x, ws);
  k_out<<<256, 512, O_BYTES, stream>>>(x, ws, (char*)d_out);
}
```

```cpp
#define NOPIN 1
#define GROUPLD 1
#include <hip/hip_runtime.h>
#include <hip/hip_bf16.h>
#include <stdint.h>

typedef __attribute__((ext_vector_type(8))) __bf16 bf16x8;
typedef __attribute__((ext_vector_type(2))) __bf16 bf16x2;
typedef __attribute__((ext_vector_type(16))) __bf16 bf16x16;
typedef __attribute__((ext_vector_type(4))) short s16x4;
typedef __attribute__((ext_vector_type(16))) float f32x16;
typedef __attribute__((ext_vector_type(8))) float f32x8;
typedef __attribute__((ext_vector_type(4))) float f32x4;
typedef __attribute__((ext_vector_type(2))) float f32x2;
typedef __attribute__((ext_vector_type(4))) unsigned u32x4;
typedef __attribute__((ext_vector_type(2))) unsigned u32x2;
typedef unsigned short u16;
#define LDS_AS __attribute__((address_space(3)))
#define LDS(T, addr) (*(LDS_AS T*)(size_t)(addr))

constexpr int S = 2048, D = 640;
constexpr float QSCALE = 0.22360679774997896f * 1.4426950408889634f;

constexpr unsigned WS_OF = 0;
constexpr unsigned WS_ZT = WS_OF + S * D * 2;
constexpr unsigned WS_UT = WS_ZT + S * D * 2;
constexpr unsigned WS_KV = WS_UT + S * D * 2 + 4096;
constexpr unsigned WS_G = WS_KV + 32 * 7168;
constexpr unsigned WS_EP = WS_G + 8192;
constexpr unsigned WS_FA = WS_EP + 256;
constexpr unsigned WS_FD = WS_FA + 256 * 64;
constexpr unsigned WS_FU = WS_FD + 1024;
constexpr unsigned WS_FE = WS_FU + 256 * 64;
constexpr unsigned WS_HO = WS_FE + 1024;
constexpr unsigned WS_HZ = WS_HO + 256 * 64;
constexpr unsigned EMPTY = 0xffffffffu;
constexpr int SPIN_MAX = 1 << 15;

constexpr int P_X = 0;
constexpr int P_WK = 81920;
constexpr int P_SA = P_WK + 25600;
constexpr int P_SB = P_SA + 20480;
constexpr int P_GW = P_SB + 20480;
constexpr int P_W2 = 2 * 14336;
constexpr int P_KV = 0;
constexpr int P_US = P_W2 + 3 * 20480;
constexpr int P_KP = P_GW + 2048;
constexpr int CHUNK = 14336;
constexpr int M_TOP = P_GW;
constexpr int CV_H = 4160, CV_U = 4640, CV_CH = 4 * CV_H + 2 * CV_U;
constexpr int M_CV = 3 * CHUNK + 2 * CV_CH;
constexpr int M_RED = M_CV + CV_CH;
constexpr int O_WE = M_TOP - 15 * 5120, O_WL = 3 * CHUNK - 3 * 5120;
constexpr int O_Y = 0;
constexpr int O_Z = 3 * CHUNK;
constexpr int L_CNT = M_TOP + CHUNK;
constexpr int L_PV = L_CNT + 64;
constexpr int L_G = L_PV + 256;
constexpr int L_BYTES = L_G + 256;
__host__ __device__ constexpr int w_at(int ks) {
  return ks < 7 ? O_WE + ks * 5120 : (ks < 10 ? O_WL + (ks - 7) * 5120 : O_WE + (ks - 3) * 5120);
}
__device__ __forceinline__ unsigned ring_buf(int c) { return (c & 3) ? ((c & 3) - 1) * CHUNK : M_TOP; }

#define STAMP_DECL
#define STAMP(i)
#define STAMPA(i)
#define STAMP_FLUSH()
#define ST_PARAM
#define ST_ARG
#define SBAR() __builtin_amdgcn_sched_barrier(0)
#define PIN(x) asm volatile("" : "+v"(x))
#define WAIT_VM(N) asm volatile("s_waitcnt vmcnt(" #N ")" ::: "memory")
#define WAIT_BAR(N) asm volatile("s_waitcnt vmcnt(" #N ") lgkmcnt(0)\n\ts_barrier" ::: "memory")
#define BAR_ONLY() asm volatile("s_waitcnt lgkmcnt(0)\n\ts_barrier" ::: "memory")
#define BAR_RAW() asm volatile("s_barrier" ::: "memory")

__device__ __forceinline__ unsigned cvtpk(float lo, float hi) {
  return __builtin_bit_cast(unsigned, __builtin_convertvector(f32x2{lo, hi}, bf16x2));
}
__device__ __forceinline__ float bf_lo(unsigned w) { return __uint_as_float(w << 16); }
__device__ __forceinline__ float bf_hi(unsigned w) { return __uint_as_float(w & 0xffff0000u); }
__device__ __forceinline__ float rbf(float x) { return bf_lo(cvtpk(x, x)); }

__device__ __forceinline__ float dot2(unsigned a, unsigned b, float c) {
  return __builtin_amdgcn_fdot2_f32_bf16(__builtin_bit_cast(bf16x2, a), __builtin_bit_cast(bf16x2, b), c, false);
}

__device__ __forceinline__ unsigned nz32(unsigned v) { return v < 0xfffeffffu ? v : 0xfffeffffu; }
__device__ __forceinline__ u32x2 pack4(float a, float b, float c, float d) { return u32x2{nz32(cvtpk(a, b)), nz32(cvtpk(c, d))}; }
__device__ __forceinline__ unsigned umax3(unsigned a, unsigned b, unsigned c) { return max(max(a, b), c); }

#define GL_ "global_load_lds_dwordx4 "
#define M0_ "s_mov_b32 m0, %0\n\ts_nop 0\n\t"
#define GLDS1(C, base, off, lds) asm volatile(M0_ GL_ "%1, %2" C :: "s"(lds), "v"(off), "s"(base) : "memory")
#define GLDS2(C, base, off, lds) \
  asm volatile(M0_ GL_ "%1, %2" C "\n\t" GL_ "%1, %2 offset:1024" C :: "s"(lds), "v"(off), "s"(base) : "memory")
#define GLDS3(C, base, off, lds)                                                                          \
  asm volatile(M0_ GL_ "%1, %2" C "\n\t" GL_ "%1, %2 offset:1024" C "\n\t" GL_ "%1, %2 offset:2048" C    \
               :: "s"(lds), "v"(off), "s"(base) : "memory")
#define GLDS5(base, o, lds)                                                                                         \
  asm volatile(M0_ GL_ "%1, %6 offset:-1024\n\t" GL_ "%2, %6\n\t" GL_ "%3, %6 offset:1024\n\t" GL_ "%4, %6 offset:2048\n\t" \
               GL_ "%5, %6 offset:3072" :: "s"(lds), "v"(o[0]), "v"(o[1]), "v"(o[2]), "v"(o[3]), "v"(o[4]), "s"(base) : "memory")
#define GLDS_WORD(base, off, lds) \
  asm volatile(M0_ "global_load_lds_dword %1, %2 sc1" :: "s"(lds), "v"(off), "s"(base) : "memory")

__device__ __forceinline__ void st16c(void* base, unsigned off, u32x4 v) {
  asm volatile("global_store_dwordx4 %0, %1, %2 sc1\n\ts_nop 1" :: "v"(off), "v"(v), "s"(base) : "memory");
}
__device__ __forceinline__ void st8c(void* base, unsigned off, u32x2 v) {
  asm volatile("global_store_dwordx2 %0, %1, %2 sc1" :: "v"(off), "v"(v), "s"(base) : "memory");
}
__device__ __forceinline__ void st4c(void* base, unsigned off, unsigned v) {
  asm volatile("global_store_dword %0, %1, %2 sc1" :: "v"(off), "v"(v), "s"(base) : "memory");
}
__device__ __forceinline__ unsigned ld4c_wait(const void* base, unsigned off) {
  unsigned v;
  asm volatile("global_load_dword %0, %1, %2 sc1\n\ts_waitcnt vmcnt(0)" : "=&v"(v) : "v"(off), "s"(base) : "memory");
  return v;
}
__device__ __forceinline__ u32x4 ld16c_wait(const void* base, unsigned off) {
  u32x4 v;
  asm volatile("global_load_dwordx4 %0, %1, %2 sc1\n\ts_waitcnt vmcnt(0)" : "=&v"(v) : "v"(off), "s"(base) : "memory");
  return v;
}
__device__ __forceinline__ void poll(const char* ws, unsigned off, unsigned tag) {
#pragma unroll 1
  for (int i = 0; i < SPIN_MAX; ++i) {
    if (__all(ld4c_wait(ws, off) == tag)) break;
    __builtin_amdgcn_s_sleep(1);
  }
}
__device__ __forceinline__ unsigned lds_add1(unsigned addr) {
  unsigned r;
  asm volatile("ds_add_rtn_u32 %0, %1, %2\n\ts_waitcnt lgkmcnt(0)" : "=&v"(r) : "v"(addr), "v"(1u) : "memory");
  return r;
}
__device__ __forceinline__ bf16x8 lds_b128(unsigned p) { return LDS(const bf16x8, p); }
__device__ __forceinline__ s16x4 lds_tr(unsigned p) {
  return __builtin_amdgcn_ds_read_tr16_b64_v4i16((LDS_AS s16x4*)(size_t)p);
}
__device__ __forceinline__ bf16x8 cat4(s16x4 a, s16x4 b) {
  return __builtin_bit_cast(bf16x8, __builtin_shufflevector(a, b, 0, 1, 2, 3, 4, 5, 6, 7));
}
#define MFMA16(a, b, c) __builtin_amdgcn_mfma_f32_16x16x32_bf16(a, b, c, 0, 0, 0)
#define MFMA32(a, b, c) __builtin_amdgcn_mfma_f32_32x32x16_bf16(a, b, c, 0, 0, 0)
#define MFMA8(a, b, c) __builtin_amdgcn_mfma_f32_32x32x8bf16_1k(a, b, c, 0, 0, 0)
#define ZERO16 f32x16{0.f, 0.f, 0.f, 0.f, 0.f, 0.f, 0.f, 0.f, 0.f, 0.f, 0.f, 0.f, 0.f, 0.f, 0.f, 0.f}
#define ZERO4 f32x4{0.f, 0.f, 0.f, 0.f}
__device__ __forceinline__ int div10(int n) { return (n * 205) >> 11; }

struct Args {
  const char *x, *kin, *vin, *shk, *hk, *sd, *wq, *wk, *wv, *wo, *gw;
  char *ws, *out;
};

__device__ __forceinline__ f32x16 pv_lo(bf16x8 v, f32x16 p, f32x16 o, int idx) {
  const f32x8 h = __builtin_shufflevector(p, p, 0, 1, 2, 3, 4, 5, 6, 7);
  return __builtin_amdgcn_smfmac_f32_32x32x32_bf16(v, __builtin_bit_cast(bf16x16, h), o, idx, 0, 0);
}
__device__ __forceinline__ f32x16 pv_hi(bf16x8 v, f32x16 p, f32x16 o, int idx) {
  const f32x8 h = __builtin_shufflevector(p, p, 8, 9, 10, 11, 12, 13, 14, 15);
  return __builtin_amdgcn_smfmac_f32_32x32x32_bf16(v, __builtin_bit_cast(bf16x16, h), o, idx, 0, 0);
}
#define EX2(v) __builtin_amdgcn_exp2f(v)
__device__ __forceinline__ s16x4 lo4(bf16x8 v) {
  return __builtin_bit_cast(s16x4, __builtin_shufflevector(v, v, 0, 1, 2, 3));
}
__device__ __forceinline__ s16x4 hi4(bf16x8 v) {
  return __builtin_bit_cast(s16x4, __builtin_shufflevector(v, v, 4, 5, 6, 7));
}
__device__ __forceinline__ float xhalf_max(float m) {
  auto r = __builtin_amdgcn_permlane32_swap(__float_as_uint(m), __float_as_uint(m), false, false);
  return fmaxf(__uint_as_float(r[0]), __uint_as_float(r[1]));
}
__device__ __forceinline__ float max16(const f32x16& s) {
  float a = fmaxf(fmaxf(s[0], s[1]), s[2]), b = fmaxf(fmaxf(s[3], s[4]), s[5]);
  a = fmaxf(fmaxf(a, s[6]), s[7]); b = fmaxf(fmaxf(b, s[8]), s[9]);
  a = fmaxf(fmaxf(a, s[10]), s[11]); b = fmaxf(fmaxf(b, s[12]), s[13]);
  a = fmaxf(fmaxf(a, s[14]), s[15]);
  return fmaxf(a, b);
}

template <int J>
__device__ __forceinline__ void conv_run(int n, f32x4& hi, f32x4& lo, unsigned ap, unsigned bp) {
  bf16x8 A[8 - J], B[8 - J];
#pragma unroll
  for (int a = 0; a < 8 - J; ++a) {
    A[a] = lds_b128(ap + 512 * a);
    B[a] = lds_b128(bp + 512 * a);
  }
#pragma unroll 1
  for (int r = 1; r < n; ++r) {
    ap += 64;
    bp += 64;
    PIN(ap); PIN(bp);
#pragma unroll
    for (int a = 0; a < 8 - J; ++a) {
      hi = MFMA16(A[a], B[a], hi);
      if (a <= J) lo = MFMA16(A[a], B[a + 7 - 2 * J], lo);
      PIN(hi);
      PIN(lo);
      A[a] = lds_b128(ap + 512 * a);
      B[a] = lds_b128(bp + 512 * a);
      SBAR();
    }
  }
#pragma unroll
  for (int a = 0; a < 8 - J; ++a) {
    hi = MFMA16(A[a], B[a], hi);
    if (a <= J) lo = MFMA16(A[a], B[a + 7 - 2 * J], lo);
  }
}

struct ConvLane { unsigned aa, ba, zoff; int jj0; };
__device__ __forceinline__ ConvLane conv_lane(int l) {
  const int i16 = l & 15, g = l >> 4;
  ConvLane c;
  c.aa = M_CV + (i16 & 3) * CV_H + 16 * ((i16 >> 2) + g);
  c.ba = M_CV + 4 * CV_H + (i16 & 1) * CV_U + 2 * (2016 - 32 * (i16 >> 1) + 8 * g) - 512 * 7;
  c.jj0 = 30 - 2 * (i16 & 3) - 8 * (i16 >> 2) - 8 * g;
  c.zoff = (32 * (i16 >> 1) + 8 * g + 4 * (i16 & 1)) * 2;
  return c;
}
__device__ __forceinline__ void conv_part(const ConvLane& cl, int J, int c, int r0, int n, unsigned db, f32x4& hi,
                                          f32x4& lo) {
  unsigned ap = cl.aa - c * CV_CH + 64 * r0, bp = cl.ba + 512 * J - c * CV_CH + 64 * r0;
  PIN(ap); PIN(bp);
  if (r0 == 0) {
    const u32x4 dm = {cl.jj0 == 0 ? db : 0u, cl.jj0 == 2 ? db : 0u, cl.jj0 == 4 ? db : 0u, cl.jj0 == 6 ? db : 0u};
    const bf16x8 df = __builtin_bit_cast(bf16x8, dm);
    hi = MFMA16(df, lds_b128(bp), ZERO4);
    lo = MFMA16(df, lds_b128(bp + 512 * (7 - 2 * J)), ZERO4);
  }
  if (J == 0) conv_run<0>(n, hi, lo, ap, bp);
  else if (J == 1) conv_run<1>(n, hi, lo, ap, bp);
  else if (J == 2) conv_run<2>(n, hi, lo, ap, bp);
  else conv_run<3>(n, hi, lo, ap, bp);
}
__device__ __forceinline__ void conv_store(char* zb, const ConvLane& cl, int J, f32x4 hi, f32x4 lo) {
  st8c(zb + 512 * (7 - J), cl.zoff, pack4(hi[0], hi[1], hi[2], hi[3]));
  st8c(zb + 512 * J, cl.zoff, pack4(lo[0], lo[1], lo[2], lo[3]));
}

struct KvLane { unsigned x[4], w; };
__device__ __forceinline__ KvLane kv_lane(int hg, int j, int l) {
  const int i16 = l & 15, g = l >> 4;
  KvLane k;
  const unsigned xa = P_X + (32 * (hg & 1) + 16 * (j & 1) + i16) * 1280 + 16 * (g ^ i16);
#pragma unroll
  for (int i = 0; i < 4; ++i) { k.x[i] = xa ^ (64 * i); PIN(k.x[i]); }
  k.w = P_WK + (8 * g + (i16 >> 2)) * 40 + 32 * (j >> 1) + 8 * (l & 3);
  PIN(k.w);
  return k;
}
template <int KS0>
__device__ __forceinline__ f32x4 kv_half(const KvLane& k, int hg) {
  bf16x8 wf[10], xf[10];
#pragma unroll
  for (int i = 0; i < 10; ++i) {
    wf[i] = cat4(lds_tr(k.w + (KS0 + i) * 1280), lds_tr(k.w + (KS0 + i) * 1280 + 160));
    xf[i] = lds_b128(k.x[(KS0 + i) & 3] + 256 * ((KS0 + i) >> 2));
  }
  f32x4 acc = ZERO4, acc2 = ZERO4;
  if (hg < 2) {
#pragma unroll
    for (int i = 0; i < 10; i += 2) { acc = MFMA16(wf[i], xf[i], acc); acc2 = MFMA16(wf[i + 1], xf[i + 1], acc2); }
  } else {
#pragma unroll
    for (int i = 0; i < 10; i += 2) { acc = MFMA16(xf[i], wf[i], acc); acc2 = MFMA16(xf[i + 1], wf[i + 1], acc2); }
  }
  return acc + acc2;
}

template <int KH>
__device__ __forceinline__ void out_phase(const Args& a, unsigned tag, int qt, int w, int l, const u32x2 (&xv)[3], int srel,
                                          unsigned oa ST_PARAM) {
  constexpr int NT = KH ? 2 : 3, T0 = KH ? 3 : 0;
  constexpr int WB = KH ? w_at(10) : O_WL;
  constexpr int WB2 = O_WE;
  const int st = w & 3, i16 = l & 15, g = l >> 4, q4 = i16 >> 2, p4 = l & 3;
  const char* of = a.ws + WS_OF + qt * 81920 + KH * 40960 + st * 1024;
  unsigned ol = l * 16;
  unsigned wa0 = (8 * g + 4 * (g & 1) + q4) * 160 + 8 * p4, wa1 = (8 * g + 4 * (1 - (g & 1)) + q4) * 160 + 8 * p4;
  unsigned wb0 = wa0 + WB2, wb1 = wa1 + WB2;
  wa0 += WB;
  wa1 += WB;
  unsigned za = O_Z + (4 * g + q4) * 128 + 32 * st + 8 * p4 + T0 * 2048;
  unsigned ra = O_Y + l * 16;
  PIN(ol); PIN(wa0); PIN(wa1); PIN(wb0); PIN(wb1); PIN(za); PIN(ra);
  u32x4 o_[10];
  bf16x8 wf[20];
#define W_AD(x, y, ks) ((KH || (ks) >= 7) ? (x) + (w_at(10 * KH + (ks)) - WB) : (y) + (w_at(ks) - WB2))
#define W_RD(k0, n)                                                                                       \
  _Pragma("unroll") for (int i = 0; i < (n) * 5; ++i)                                                     \
      wf[i] = cat4(lds_tr(W_AD(wa0, wb0, (k0) + i / 5) + (i % 5) * 32), lds_tr(W_AD(wa1, wb1, (k0) + i / 5) + (i % 5) * 32)); \
  SBAR();
#define O_MM(k0, n)                                                                                       \
  _Pragma("unroll") for (int i = 0; i < (n) * 5; ++i)                                                     \
      acc[i % 5] = MFMA16(wf[i], __builtin_bit_cast(bf16x8, o_[(k0) + i / 5]), acc[i % 5]);               \
  asm volatile("" : "+v"(acc[0]), "+v"(acc[1]), "+v"(acc[2]), "+v"(acc[3]), "+v"(acc[4]));               \
  SBAR();
#define O_LD(i) asm volatile("global_load_dwordx4 %0, %1, %2 sc1" : "=&v"(o_[i]) : "v"(ol), "s"(of + (i) * 4096) : "memory")
  const float gt = LDS(const float, L_G + srel * 4);
  f32x4 gx[NT];
#pragma unroll
  for (int t = 0; t < NT; ++t) {
    gx[t] = f32x4{gt * bf_lo(xv[t][0]), gt * bf_hi(xv[t][0]), gt * bf_lo(xv[t][1]), gt * bf_hi(xv[t][1])};
    PIN(gx[t]);
  }
  float og = rbf(1.0f - gt);
  PIN(og);
  STAMP(8);
  if (w == 0) {
    poll(a.ws, WS_HO + ((qt >> 2) + 8 * (qt & 3) + 32 * (l & 7)) * 64, tag);
    *(volatile LDS_AS unsigned*)(size_t)(L_CNT + 32) = 1u;
  } else {
#pragma unroll 1
    for (int i = 0; i < SPIN_MAX * 16; ++i) {
      if (__builtin_amdgcn_readfirstlane(*(volatile LDS_AS unsigned*)(size_t)(L_CNT + 32)) != 0) break;
      __builtin_amdgcn_s_sleep(1);
    }
  }
  STAMP(9);
  f32x4 acc[5];
#define O_MAX(k0, n) \
  _Pragma("unroll") for (int k = (k0); k < (k0) + (n); ++k) m = umax3(umax3(m, o_[k][0], o_[k][1]), o_[k][2], o_[k][3]);
#pragma unroll 1
  for (int pass = 0; pass < SPIN_MAX; ++pass) {
#pragma unroll
    for (int i = 0; i < 10; ++i) O_LD(i);
#pragma unroll
    for (int i = 0; i < 5; ++i) acc[i] = ZERO4;
    unsigned m = 0;
    W_RD(0, 4);
    asm volatile("s_waitcnt vmcnt(6)" : "+v"(o_[0]), "+v"(o_[1]), "+v"(o_[2]), "+v"(o_[3]) :: "memory");
    O_MAX(0, 4);
    O_MM(0, 4);
    W_RD(4, 3);
    asm volatile("s_waitcnt vmcnt(3)" : "+v"(o_[4]), "+v"(o_[5]), "+v"(o_[6]) :: "memory");
    O_MAX(4, 3);
    O_MM(4, 3);
    asm volatile("s_waitcnt vmcnt(0)" : "+v"(o_[7]), "+v"(o_[8]), "+v"(o_[9]) :: "memory");
#pragma unroll 1
    for (int i = 0; i < SPIN_MAX * 16; ++i) {
      if (__builtin_amdgcn_readfirstlane(*(volatile LDS_AS unsigned*)(size_t)(L_CNT + 4)) == 4 &&
          __builtin_amdgcn_readfirstlane(*(volatile LDS_AS unsigned*)(size_t)(L_CNT + 8)) == 8) break;
      __builtin_amdgcn_s_sleep(1);
    }
    STAMP(10);
    W_RD(7, 3);
    O_MAX(7, 3);
    O_MM(7, 3);
    if (!__any(m == EMPTY)) break;
  }
  STAMP(11);
#pragma unroll
  for (int t = 0; t < 5 - NT; ++t) LDS(f32x4, ra + w * 3072 + t * 1024) = acc[(KH ? 0 : 3) + t];
  BAR_ONLY();
  STAMP(13);
#pragma unroll
  for (int t = 0; t < NT; ++t) {
    const f32x4 b = acc[T0 + t] + LDS(const f32x4, ra + (w ^ 4) * 3072 + t * 1024);
    const u32x2 z = __builtin_bit_cast(u32x2, lds_tr(za + t * 2048));
    *(u32x2*)(a.out + oa + t * 32) =
        u32x2{cvtpk(fmaf(og, b[0], gx[t][0] * bf_lo(z[0])), fmaf(og, b[1], gx[t][1] * bf_hi(z[0]))),
              cvtpk(fmaf(og, b[2], gx[t][2] * bf_lo(z[1])), fmaf(og, b[3], gx[t][3] * bf_hi(z[1])))};
  }
}

__device__ __forceinline__ void mat_wave(const Args& a, int bid, int qt, int hg, int w, int l) {
  char* ws = a.ws;
  STAMP_DECL; STAMP(0);
  const int h = l >> 5, hd = w & 3, kh = w >> 2;
  {
    const char* xb = a.x + qt * 81920 + w * 10240;
#pragma unroll
    for (int i = 0; i < 10; ++i) {
      const int n = i * 64 + l;
      const int row = (n * 205) >> 14, sl = n - row * 80;
      GLDS1("", xb, row * 1280 + 16 * (sl ^ ((8 * w + row) & 15)), P_X + w * 10240 + i * 1024);
    }
  }
  unsigned wqo[3];
#pragma unroll
  for (int i = 0; i < 3; ++i) {
    int n = 64 * i + l;
    n = n < 160 ? n : 159;
    const int pr = div10(n), cc = n - pr * 10;
    wqo[i] = (16 * w + (pr ^ ((pr >> 1) & 4))) * 1280 + 16 * cc - 1024 * i;
  }
#define WQ_ISSUE(c, slot)                                                                                        \
  {                                                                                                              \
    const char* b_ = a.wq + hg * 160 + (c) * 128 * 1280;                                                         \
    asm volatile(M0_ GL_ "%1, %3\n\t" GL_ "%2, %3 offset:1024" :: "s"((slot) + w * 2560), "v"(wqo[0]), "v"(wqo[1]), "s"(b_) : "memory"); \
    if (l < 32) asm volatile(GL_ "%0, %1 offset:2048" :: "v"(wqo[2]), "s"(b_) : "memory");                       \
  }
  WQ_ISSUE(0, P_SA);
  WQ_ISSUE(1, P_SB);
  unsigned oo0, oo1, oo2;
  {
    const int q = l & 31;
    const int col = (q & 8) | ((q & 1) << 2) | ((q & 7) >> 1);
    const unsigned ob = (2 * kh + (q >> 4)) * 1024 + col * 16;
    const int k0 = (hg * 4 + hd) * 20 + 4 * h;
#define O_OFF(k) (ob + ((k) >> 5) * 4096 + (((k) >> 3) & 3) * 256 + ((k) & 7) * 2)
    oo0 = O_OFF(k0); oo1 = O_OFF(k0 + 8); oo2 = O_OFF(k0 + 16);
#undef O_OFF
    PIN(oo0); PIN(oo1); PIN(oo2);
  }
  int srel = 16 * hd + ((l & 8) | ((l & 3) << 1) | ((l >> 2) & 1));
  unsigned xe[3];
#pragma unroll
  for (int t = 0; t < 3; ++t) {
    const int cb = hg * 160 + (48 * kh + 16 * (kh && t == 2 ? 0 : t) + 4 * (l >> 4)) * 2;
    xe[t] = P_X + srel * 1280 + (cb & ~255) + ((((cb >> 4) & 15) ^ (srel & 15)) << 4) + (cb & 8);
    PIN(xe[t]);
  }
  PIN(srel);
  unsigned xa[4], wa0, wa1, qs[3];
  {
    const int i16 = l & 15, g = l >> 4, q4 = i16 >> 2, p4 = l & 3;
#pragma unroll
    for (int i = 0; i < 4; ++i) { xa[i] = P_X + (16 * hd + i16) * 1280 + 16 * ((4 * i + g) ^ i16); PIN(xa[i]); }
    wa0 = P_W2 + (8 * g + 4 * (g & 1) + q4) * 160 + 96 * kh + 8 * p4;
    wa1 = P_W2 + (8 * g + 4 * (1 - (g & 1)) + q4) * 160 + 96 * kh + 8 * p4;
    PIN(wa0); PIN(wa1);
#pragma unroll
    for (int t = 0; t < 3; ++t) {
      const int gam = 12 * kh + 4 * t + g, hq = (gam * 13) >> 6, sub = gam - 5 * hq, q = 16 * (hd & 1) + i16;
      qs[t] = P_SA + (hq + 4 * (hd >> 1)) * 1536 + (sub < 4 ? (q + 32 * (sub & 1)) * 16 + 8 * (sub >> 1) : 1024 + q * 8);
      PIN(qs[t]);
    }
  }
  const KvLane kl = kv_lane(hg, hd, l);
  unsigned gxa, gwa;
  {
    const int grow = 8 * hg + 2 * hd + h, gi = l & 31;
    gxa = P_X + grow * 1280 + 16 * ((gi >> 1) ^ (grow & 15)) + 8 * (gi & 1);
    gwa = P_GW + gi * 8;
    PIN(gxa); PIN(gwa);
  }
  STAMPA(6);
  WAIT_BAR(6);
  STAMPA(7);
  if (kh == 0) {
    if (hg < 4) LDS(f32x4, P_KP + hd * 1024 + l * 16) = kv_half<10>(kl, hg);
  } else {
    u32x2 gx[5], gv[5];
#pragma unroll
    for (int c = 0; c < 5; ++c) {
      gx[c] = LDS(const u32x2, gxa + c * 256);
      gv[c] = LDS(const u32x2, gwa + c * 256);
    }
    float g0 = 0.f, g1 = 0.f;
#pragma unroll
    for (int c = 0; c < 5; ++c) {
      g0 = dot2(gx[c][0], gv[c][0], g0);
      g1 = dot2(gx[c][1], gv[c][1], g1);
    }
    float gacc = g0 + g1;
    gacc += __int_as_float(__builtin_amdgcn_update_dpp(0, __float_as_int(gacc), 0x111, 0xf, 0xf, true));
    gacc += __int_as_float(__builtin_amdgcn_update_dpp(0, __float_as_int(gacc), 0x112, 0xf, 0xf, true));
    gacc += __int_as_float(__builtin_amdgcn_update_dpp(0, __float_as_int(gacc), 0x114, 0xf, 0xf, true));
    gacc += __int_as_float(__builtin_amdgcn_update_dpp(0, __float_as_int(gacc), 0x118, 0xf, 0xf, true));
    gacc += __int_as_float(__builtin_amdgcn_update_dpp(0, __float_as_int(gacc), 0x142, 0xa, 0xf, true));
    const float sg = rbf(__builtin_amdgcn_rcpf(1.0f + __expf(-rbf(gacc))));
    if ((l & 31) == 31) LDS(float, P_KP + 4096 + (2 * hd + h) * 4) = sg;
  }
  STAMPA(8);
  BAR_ONLY();
  u32x2 xv[3];
  bf16x8 xf[20], wf[12];
#pragma unroll
  for (int t = 0; t < 3; ++t) xv[t] = LDS(const u32x2, xe[t]);
#pragma unroll
  for (int ks = 0; ks < 20; ++ks) xf[ks] = lds_b128(xa[ks & 3] + 256 * (ks >> 2));
#define MEET(N, total)                                                                                     \
  asm volatile("s_waitcnt vmcnt(" #N ") lgkmcnt(0)" ::: "memory");                                         \
  if (l == 0) asm volatile("ds_add_u32 %0, %1" :: "v"(L_CNT + 40), "v"(1u) : "memory");                    \
  _Pragma("unroll 1") for (int i = 0; i < SPIN_MAX * 16; ++i)                                              \
    if (__builtin_amdgcn_readfirstlane(*(volatile LDS_AS unsigned*)(size_t)(L_CNT + 40)) >= (total)) break;
  f32x4 qa[3] = {ZERO4, ZERO4, ZERO4};
#define Q_CHUNK(c, b0, b1, slot, NT)                                                                        \
  {                                                                                                         \
    _Pragma("unroll") for (int i = 0; i < 4 * NT; ++i)                                                      \
        wf[i] = cat4(lds_tr((b0) + (slot) + (i / NT) * 5120 + (i % NT) * 32), lds_tr((b1) + (slot) + (i / NT) * 5120 + (i % NT) * 32)); \
    SBAR();                                                                                                 \
    _Pragma("unroll") for (int i = 0; i < 4 * NT; ++i) qa[i % NT] = MFMA16(wf[i], xf[4 * (c) + i / NT], qa[i % NT]); \
    asm volatile("" : "+v"(qa[0]), "+v"(qa[1]), "+v"(qa[2]));                                               \
    SBAR();                                                                                                 \
  }
#define Q_ALL(NT)                                                                              \
  WQ_ISSUE(2, P_W2);                                                                           \
  Q_CHUNK(0, wb0, wb1, 0, NT);                                                                 \
  WQ_ISSUE(3, P_W2 + 20480);                                                                   \
  WQ_ISSUE(4, P_W2 + 40960);                                                                   \
  if (l == 0) asm volatile("ds_add_u32 %0, %1" :: "v"(L_CNT + 44), "v"(1u) : "memory");        \
  Q_CHUNK(1, wb0, wb1, 20480, NT);                                                             \
  MEET(6, 8);                                                                                  \
  STAMPA(10);                                                                                  \
  Q_CHUNK(2, wa0, wa1, 0, NT);                                                                 \
  MEET(0, 16);                                                                                 \
  STAMPA(12);                                                                                  \
  Q_CHUNK(3, wa0, wa1, 20480, NT);                                                             \
  Q_CHUNK(4, wa0, wa1, 40960, NT);
  WAIT_BAR(0);
  PIN(xv[0]); PIN(xv[1]); PIN(xv[2]);
  STAMP(1);
  {
    unsigned wb0 = wa0 + (P_SA - P_W2), wb1 = wa1 + (P_SA - P_W2);
    PIN(wb0); PIN(wb1);
    if (kh == 0) { Q_ALL(3); } else { Q_ALL(2); }
  }
  STAMP(2);
  const int idx = 0xdddd;
  const f32x16 zero = ZERO16;
  f32x16 O1 = zero, O2 = zero, SA, SB, SC, SD = zero;
  PIN(O1); PIN(O2); PIN(SD);
#pragma unroll
  for (int t = 0; t < 3; ++t)
    if (t < 2 || kh == 0) LDS(u32x2, qs[t]) = u32x2{cvtpk(qa[t][0] * QSCALE, qa[t][1] * QSCALE), cvtpk(qa[t][2] * QSCALE, qa[t][3] * QSCALE)};
  STAMP(3);
  BAR_ONLY();
  STAMP(5);
  const bf16x8 qa1 = lds_b128(P_SA + w * 1536 + l * 16);
  const bf16x8 qa2 = lds_b128(P_SA + (w ^ 4) * 1536 + l * 16);
  u32x2 q81 = LDS(const u32x2, P_SA + w * 1536 + 1024 + (l & 31) * 8);
  u32x2 q82 = LDS(const u32x2, P_SA + (w ^ 4) * 1536 + 1024 + (l & 31) * 8);
  if (h == 1) {
    q81 = u32x2{0u, 0u};
    q82 = u32x2{0u, 0u};
  }
  const unsigned tag = __builtin_amdgcn_readfirstlane(LDS(const unsigned, L_CNT + 28));
  const unsigned ra = kh * 7168 + l * 16;
#define LDF(c, off) lds_b128(ra + (c) + (off))
  bf16x8 k16 = LDF(M_TOP, 0), k8p = LDF(M_TOP, 2048), v0 = LDF(M_TOP, 3072), v1 = v0;
  {
    const bf16x8 k0 = lds_b128(M_TOP + l * 16);
    const s16x4 e0 = lo4(lds_b128(M_TOP + 2048 + l * 16));
    SA = MFMA8(e0, __builtin_bit_cast(s16x4, q81), MFMA32(k0, qa1, zero));
    SB = MFMA8(e0, __builtin_bit_cast(s16x4, q82), MFMA32(k0, qa2, zero));
    SC = MFMA32(k16, qa1, zero);
    f32x16 T = MFMA32(k16, qa2, zero);
    const float m1 = xhalf_max(max16(SA)), m2 = xhalf_max(max16(SB));
    if (h == 1) {
      q81[0] = cvtpk(-m1, 0.f);
      q82[0] = cvtpk(-m2, 0.f);
    }
    SA = MFMA8(lo4(k8p), __builtin_bit_cast(s16x4, q81), SC);
    SB = MFMA8(lo4(k8p), __builtin_bit_cast(s16x4, q82), T);
  }
  const s16x4 qb1 = __builtin_bit_cast(s16x4, q81), qb2 = __builtin_bit_cast(s16x4, q82);
  k16 = LDF(M_TOP, 1024);
#define PINX(X)
#define E4(X, i)                                                                                   \
  X[i] = EX2(X[i]); X[i + 1] = EX2(X[i + 1]); X[i + 2] = EX2(X[i + 2]); X[i + 3] = EX2(X[i + 3]); \
  PINX(X); SBAR();
#define E2(X, i) X[i] = EX2(X[i]); X[i + 1] = EX2(X[i + 1]); PINX(X); SBAR();
#define APRIO(n) (n)
#define PRIO(n) __builtin_amdgcn_s_setprio(APRIO(n)); SBAR();
#define HALF1(C1, N1, N2, K8, V1P)                                               \
  {                                                                              \
    E2(C1, 0); O2 = pv_hi(V1P, N2, O2, idx); SBAR();                             \
    E4(C1, 2); N1 = MFMA32(k16, qa1, zero); SBAR();                              \
    E4(C1, 6); N1 = MFMA8(K8, qb1, N1); SBAR();                                  \
    E4(C1, 10); O1 = pv_lo(v0, C1, O1, idx); SBAR();                             \
    E2(C1, 14);                                                                  \
  }
#define HALF2(C1, C2, N2, K8, V1C, LV1N, LK16, LK8, LV0)                         \
  {                                                                              \
    E2(C2, 0); O1 = pv_hi(V1C, C1, O1, idx); LV1N; SBAR();                       \
    E4(C2, 2); N2 = MFMA32(k16, qa2, zero); SBAR();                              \
    E4(C2, 6); N2 = MFMA8(K8, qb2, N2); LK8; SBAR();                             \
    E4(C2, 10); O2 = pv_lo(v0, C2, O2, idx); v0 = LV0; SBAR(); k16 = LK16; SBAR(); \
    E2(C2, 14);                                                                  \
  }
  STAMP(4);
#define TICKSTAMP(k)
  unsigned rt = ra + M_TOP;
  PIN(rt);
#define LDR(b, off) ((b) == 0 ? lds_b128(rt + (off)) : lds_b128(ra + ((b) - 1) * CHUNK + (off)))
  bf16x8 v1b = v0;
  v1 = lds_b128(rt + 4096);
#define ITER(cur, nxt)                                                                        \
    HALF1(SA, SC, SD, hi4(k8p), v1b);                                                         \
    BAR_RAW();                                                  \
    TICKSTAMP(cur);                                                                           \
    PRIO(3);                                                                                  \
    HALF2(SA, SB, SD, hi4(k8p), v1, v1b = LDR(cur, 6144), LDR(nxt, 0), k8p = LDR(nxt, 2048), LDR(cur, 5120)); \
    PRIO(2);                                                                                  \
    HALF1(SC, SA, SB, lo4(k8p), v1);                                                          \
    PRIO(1);                                                                                  \
    HALF2(SC, SD, SB, lo4(k8p), v1b, v1 = LDR(nxt, 4096), LDR(nxt, 1024), (void)0, LDR(nxt, 3072)); \
    PRIO(0);
#pragma unroll 1
  for (int c = 0; c < 4; ++c) {
    ITER(0, 1);
    ITER(1, 2);
    ITER(2, 3);
    ITER(3, 0);
  }
  O2 = pv_hi(v1b, SD, O2, idx);
  __builtin_amdgcn_s_setprio(3);
  STAMP(6);
  {
    const unsigned sa = O_Y + w * 3328 + l * 16;
#pragma unroll
    for (int i = 0; i < 3; ++i) LDS(f32x4, sa + i * 1024) = f32x4{O2[4 * i], O2[4 * i + 1], O2[4 * i + 2], O2[4 * i + 3]};
    LDS(float, O_Y + w * 3328 + 3072 + l * 4) = O2[12];
  }
  BAR_ONLY();
  f32x4 Oa, Ob, Oc;
  float lsum;
  {
    const unsigned sa = O_Y + (w ^ 4) * 3328 + l * 16;
    Oa = f32x4{O1[0], O1[1], O1[2], O1[3]} + LDS(const f32x4, sa);
    Ob = f32x4{O1[4], O1[5], O1[6], O1[7]} + LDS(const f32x4, sa + 1024);
    Oc = f32x4{O1[8], O1[9], O1[10], O1[11]} + LDS(const f32x4, sa + 2048);
    const float l12 = O1[12] + LDS(const float, O_Y + (w ^ 4) * 3328 + 3072 + l * 4);
    lsum = h ? Oc[0] : l12;
  }
  const f32x4 nz = Oa * 0.f + Ob * 0.f + Oc * 0.f;
  const bool ok = (lsum >= 9.094947e-13f) && (lsum <= 1.0e36f) && ((nz[0] + nz[1]) + (nz[2] + nz[3]) == 0.f);
  if (__builtin_expect(__any(!ok), 0)) {
    u32x2 q8 = q81;
    if (h == 1) q8 = u32x2{0u, 0u};
    const char* kvg = ws + WS_KV;
    float m = -INFINITY;
    f32x16 O;
#pragma unroll 1
    for (int pass = 0; pass < 2; ++pass) {
      O = zero;
#pragma unroll 1
      for (int t = 0; t < 64; ++t) {
        const unsigned b = (t >> 1) * 7168 + l * 16;
        const bf16x8 k8 = __builtin_bit_cast(bf16x8, ld16c_wait(kvg, b + 2048));
        f32x16 s = MFMA8((t & 1) ? hi4(k8) : lo4(k8), __builtin_bit_cast(s16x4, q8),
                         MFMA32(__builtin_bit_cast(bf16x8, ld16c_wait(kvg, b + 1024 * (t & 1))), qa1, zero));
        m = fmaxf(m, max16(s));
#pragma unroll
        for (int i = 0; i < 16; ++i) s[i] = EX2(s[i]);
        O = pv_lo(__builtin_bit_cast(bf16x8, ld16c_wait(kvg, b + 3072 + 2048 * (t & 1))), s, O, idx);
        O = pv_hi(__builtin_bit_cast(bf16x8, ld16c_wait(kvg, b + 4096 + 2048 * (t & 1))), s, O, idx);
      }
      m = xhalf_max(m);
      const float m0 = rbf(m), m1 = rbf(m - m0), m2 = rbf(m - m0 - m1);
      if (h == 1) q8 = u32x2{cvtpk(-m0, -m1), cvtpk(-m2, 0.f)};
    }
    lsum = h ? O[8] : O[12];
    Oa = f32x4{O[0], O[1], O[2], O[3]};
    Ob = f32x4{O[4], O[5], O[6], O[7]};
    Oc = f32x4{O[8], O[9], O[10], O[11]};
  }
  const float inv = __builtin_amdgcn_rcpf(lsum);
  char* ob = ws + WS_OF + qt * 81920;
  st8c(ob, oo0, pack4(Oa[0] * inv, Oa[1] * inv, Oa[2] * inv, Oa[3] * inv));
  st8c(ob, oo1, pack4(Ob[0] * inv, Ob[1] * inv, Ob[2] * inv, Ob[3] * inv));
  if (h == 0) st8c(ob, oo2, pack4(Oc[0] * inv, Oc[1] * inv, Oc[2] * inv, Oc[3] * inv));
#define HO_AT 0
  if (l == 0 && lds_add1(L_CNT + 8) == HO_AT) st4c(ws, WS_HO + bid * 64, tag);
  __builtin_amdgcn_s_setprio(0);
  STAMP(7);
  const unsigned oa = (qt * 64 + srel) * 1280 + (hg * 80 + 48 * kh + 4 * (l >> 4)) * 2;
  if (kh == 0) out_phase<0>(a, tag, qt, w, l, xv, srel, oa ST_ARG);
  else out_phase<1>(a, tag, qt, w, l, xv, srel, oa ST_ARG);
  STAMP(12);
  STAMP_FLUSH();
}

__device__ __forceinline__ void help_wave(const Args& a, int bid, int qt, int hg, int j, int l) {
  char* ws = a.ws;
  STAMP_DECL; STAMP(0);
#define HPRIO 3
  __builtin_amdgcn_s_setprio(HPRIO);
  const int s0 = qt * 64;
  const int i16 = l & 15, g = l >> 4;
  const int chs = 5 * (bid >> 1) + 2, cha = chs - 2 + 3 * (bid & 1);
  if (hg < 4) {
    const char* kb = (hg < 2 ? a.wk : a.wv) + j * 6144;
    GLDS3("", kb, l * 16, P_WK + j * 6144);
    GLDS3("", kb + 3072, l * 16, P_WK + j * 6144 + 3072);
    if (j == 0) GLDS1("", kb + 24576, l * 16, P_WK + 24576);
  }
  if (j == 1) GLDS1("", a.gw, l * 16, P_GW);
  if (j == 2 && l < 16) GLDS1("", a.gw + 1024, l * 16, P_GW + 1024);
  if (j == 3 && l < 16) LDS(unsigned, L_CNT + l * 4) = 0u;
  const int dp = l < 40 ? l : 39;
  u32x4 sk;
  unsigned ev, hv0, hv1, hv2, dv0, dv1, dv2;
  {
    const int e0 = 2 * i16 - 30 + 2 * j;
    const unsigned ho = (e0 < 0 ? 0 : e0) * 2;
    asm volatile("global_load_dwordx4 %0, %8, %9\n\t"
                 "global_load_dword %1, %10, %11\n\tglobal_load_dword %2, %12, %11\n\tglobal_load_dword %3, %13, %11\n\t"
                 "global_load_ushort %4, %14, %15\n\tglobal_load_ushort %5, %16, %15\n\tglobal_load_ushort %6, %16, %15 offset:2\n\t"
                 "global_load_dword %7, %17, %18 sc1"
                 : "=&v"(sk), "=&v"(hv0), "=&v"(hv1), "=&v"(hv2), "=&v"(dv0), "=&v"(dv1), "=&v"(dv2), "=&v"(ev)
                 : "v"(hg * 640 + dp * 16), "s"(a.shk), "v"(ho + chs * 4096), "s"(a.hk), "v"(ho + cha * 4096),
                   "v"(ho + cha * 4096 + 4096), "v"(chs * 2), "s"(a.sd), "v"(cha * 2), "v"(WS_EP), "s"(ws) : "memory");
  }
  unsigned wo_off[5];
#pragma unroll
  for (int i = 0; i < 5; ++i) {
    const int n = 64 * i + l;
    const int pr = div10(n), cc = n - pr * 10;
    wo_off[i] = (pr ^ (((pr >> 3) & 1) << 2)) * 1280 + 16 * cc - 1024 * (i - 1);
    PIN(wo_off[i]);
  }
  const KvLane kl = kv_lane(hg, j, l);
  unsigned kvs;
  bool kvon = true;
  unsigned kvc = 0, kvd = 0, kvm = 0;
  {
    const int t = hg & 1, am = j & 1, nt = j >> 1;
    if (hg < 2) {
      const int key = 16 * am + i16;
      kvs = nt == 0 ? 1024 * t + 16 * (key + 32 * (g & 1)) + 8 * (g >> 1) : 2048 + 16 * (key + 32 * g) + 8 * t;
      kvon = nt == 0 || g < 2;
      if (nt == 1 && g == 1) { kvm = ~0u; kvc = 0x3f803f80u; kvd = 0x00003f80u; }
    } else {
      const int d = 16 * nt + i16;
      kvs = 3072 + 2048 * t + 1024 * am + 16 * (d + 32 * (g >> 1)) + 8 * (g & 1);
      if (d >= 20) { kvm = ~0u; kvc = kvd = (d == 20 || d == 24) ? 0x3f803f80u : 0u; }
    }
    kvs += WS_KV + qt * 7168;
    PIN(kvs); PIN(kvc); PIN(kvd); PIN(kvm);
  }
  unsigned ro = (j >> 1) * 16 * 7168 + (j & 1) * 4096 + l * 16;
  const unsigned rd = (j >> 1) * 7168 + (j & 1) * 4096;
  unsigned fa_off, fu_off;
  {
    const int n = l < 24 ? l : 0;
    const int qq = ((qt + (n >> 3)) & 15) + 16 * ((n >> 2) & 1);
    fa_off = WS_FA + ((qq >> 2) + 8 * (qq & 3) + 32 * (n & 3)) * 64;
    const int ch = chs - 2 + 2 * (bid & 1) + 2 * (l >> 5), q2 = l & 31;
    fu_off = WS_FU + ((q2 >> 2) + 8 * (q2 & 3) + 32 * (ch / 80)) * 64;
  }
  unsigned ho0 = (2 + 2 * j + 8 * l) * 2, ho3 = ho0 > 1008 ? 1008 : ho0;
  unsigned uo = (j < 2 ? 2 : 0) + (j & 1) * 2048 + l * 16;
  ConvLane cl = conv_lane(l);
  PIN(cl.aa); PIN(cl.ba); PIN(cl.zoff); PIN(cl.jj0);
  unsigned usrc[3], udst = WS_UT + (hg * 80 + 20 * j + (l >> 3)) * 4096 + (1984 - s0) * 2 + (l & 7) * 16;
#pragma unroll
  for (int i = 0; i < 3; ++i) {
    const int r = 20 * j + 8 * i + (l >> 3), pc = l & 7;
    usrc[i] = P_US + (3 - (pc >> 1)) * 3200 + (r >> 1) * 80 + (r & 1) * 32 + (pc & 1) * 16;
    PIN(usrc[i]);
  }
  unsigned zo = (l >> 3) * 4096 + (l & 7) * 16;
  PIN(ro); PIN(fa_off); PIN(fu_off); PIN(ho0); PIN(ho3); PIN(uo); PIN(udst); PIN(zo);
  unsigned ko[3], vo[3];
#pragma unroll
  for (int i = 0; i < 3; ++i) {
    const int n = 64 * i + l, nk = n < 189 ? n : 189, nv = n < 159 ? n : 159;
    const int rk = div10(nk), rv = div10(nv), row = s0 + 16 * j - 3 + rk;
    ko[i] = (row < 0 ? 0 : row) * 1280 + hg * 160 + (nk - rk * 10) * 16 + 1024 - 1024 * i;
    vo[i] = (s0 + 16 * j + rv) * 1280 + hg * 160 + (nv - rv * 10) * 16 + 1024 - 1024 * i;
    PIN(ko[i]); PIN(vo[i]);
  }
  asm volatile("s_waitcnt vmcnt(0) lgkmcnt(0)\n\ts_barrier"
               : "+v"(ev), "+v"(sk), "+v"(hv0), "+v"(hv1), "+v"(hv2), "+v"(dv0), "+v"(dv1), "+v"(dv2) :: "memory");
  const unsigned tag = __builtin_amdgcn_readfirstlane(ev) + 1u;
  STAMP(1);
  {
    f32x4 acc = ZERO4;
    if (hg < 4) acc = kv_half<0>(kl, hg);
    PIN(acc);
    BAR_ONLY();
    if (hg < 4) {
      acc += LDS(const f32x4, P_KP + j * 1024 + l * 16);
      const u32x2 v = {(cvtpk(acc[0], acc[1]) & ~kvm) | kvc, (cvtpk(acc[2], acc[3]) & ~kvm) | kvd};
      if (kvon) st8c(ws, kvs, v);
    }
    if (l < 2) st4c(ws, WS_G + (s0 + 8 * hg + 2 * j + l) * 4, LDS(const unsigned, P_KP + 4096 + (2 * j + l) * 4));
  }
  if (j == 0 && l == 0) LDS(unsigned, L_CNT + 28) = tag;
  STAMP(2);
  BAR_ONLY();
  WAIT_VM(0);
  if (l == 0 && lds_add1(L_CNT) == 3) {
    st4c(ws, WS_FA + bid * 64, tag);
    st4c(ws, WS_FD + bid * 4, tag);
  }
  STAMP(3);
  int next = 0;
#define RING_ISSUE()                                                       \
  {                                                                        \
    const char* b_ = ws + WS_KV + ((next + qt) & 15) * 7168;               \
    const unsigned d_ = ring_buf(next) + rd;                               \
    GLDS3(" sc1", b_, ro, d_);                                             \
    if (!(j & 1)) GLDS1(" sc1", b_ + 3072, ro, d_ + 3072);                 \
    ++next;                                                                \
  }
#define LDS_WORD(off) (*(volatile LDS_AS unsigned*)(size_t)(L_CNT + (off)))
#define GLDS3V(base, o, lds)                                                                    \
  asm volatile(M0_ GL_ "%1, %4 offset:-1024\n\t" GL_ "%2, %4\n\t" GL_ "%3, %4 offset:1024"     \
               :: "s"((lds) + 1024), "v"(o[0]), "v"(o[1]), "v"(o[2]), "s"(base) : "memory")
#pragma unroll 1
  for (int i = 0; i < SPIN_MAX * 16; ++i) {
    if (__builtin_amdgcn_readfirstlane(LDS_WORD(44)) == 8) break;
    __builtin_amdgcn_s_sleep(2);
  }
  GLDS3V(a.kin, ko, P_KV + j * 6144);
  GLDS3V(a.vin, vo, P_KV + j * 6144 + 3072);
  WAIT_VM(0);
  STAMP(7);
  if (j == 3) {
    poll(ws, fa_off, tag);
    LDS_WORD(12) = 1u;
  }
#pragma unroll 1
  for (int i = 0; i < SPIN_MAX * 16; ++i) {
    if (__builtin_amdgcn_readfirstlane(LDS_WORD(12)) != 0 && __builtin_amdgcn_readfirstlane(LDS_WORD(0)) == 4) break;
    __builtin_amdgcn_s_sleep(1);
  }
  STAMP(4);
  RING_ISSUE();
  {
    unsigned rg = P_KV + j * 6144 + dp * 4;
    PIN(rg);
    unsigned kk[19], vv[16];
#pragma unroll
    for (int r = 0; r < 19; ++r) kk[r] = LDS(const unsigned, rg + r * 160);
#pragma unroll
    for (int r = 0; r < 16; ++r) vv[r] = LDS(const unsigned, rg + 3072 + r * 160);
    if (s0 + j == 0) kk[0] = kk[1] = kk[2] = 0u;
    const f32x2 t0 = {bf_lo(sk[0]), bf_lo(sk[2])}, t1 = {bf_hi(sk[0]), bf_hi(sk[2])};
    const f32x2 t2 = {bf_lo(sk[1]), bf_lo(sk[3])}, t3 = {bf_hi(sk[1]), bf_hi(sk[3])};
#define UNP(x) f32x2{bf_lo(x), bf_hi(x)}
    f32x2 u[16];
#pragma unroll
    for (int r = 0; r < 16; ++r)
      u[r] = (UNP(kk[r + 3]) * t0 + UNP(kk[r + 2]) * t1 + UNP(kk[r + 1]) * t2 + UNP(kk[r]) * t3) * UNP(vv[r]);
    if (l < 40) {
      const unsigned sg = P_US + j * 3200 + dp * 80;
#pragma unroll
      for (int e = 0; e < 2; ++e)
#pragma unroll
        for (int o = 0; o < 2; ++o)
          LDS(u32x4, sg + 32 * e + 16 * o) =
              u32x4{cvtpk(u[15 - 8 * o][e], u[14 - 8 * o][e]), cvtpk(u[13 - 8 * o][e], u[12 - 8 * o][e]),
                    cvtpk(u[11 - 8 * o][e], u[10 - 8 * o][e]), cvtpk(u[9 - 8 * o][e], u[8 - 8 * o][e])};
    }
  }
  STAMP(5);
  WAIT_BAR(0);
  RING_ISSUE();
  if (j == 2) GLDS1(" sc1", ws + WS_FD, l * 16, P_SB);
  RING_ISSUE();
  st16c(ws, udst, LDS(const u32x4, usrc[0]));
  st16c(ws + 8 * 4096, udst, LDS(const u32x4, usrc[1]));
  if (l < 32) st16c(ws + 16 * 4096, udst, LDS(const u32x4, usrc[2]));
  STAMP(6);
  const int js = 2 * (bid & 1) + (j >> 1);
  WAIT_VM(6);
  if (j == 2) {
    const u32x4 f0 = *(volatile LDS_AS u32x4*)(size_t)(P_SB + l * 16);
    if (!__all(f0[0] == tag && f0[1] == tag && f0[2] == tag && f0[3] == tag)) {
#pragma unroll 1
      for (int i = 0; i < SPIN_MAX; ++i) {
        const u32x4 f = ld16c_wait(ws, WS_FD + l * 16);
        if (__all(f[0] == tag && f[1] == tag && f[2] == tag && f[3] == tag)) break;
        __builtin_amdgcn_s_sleep(1);
      }
    }
    if (bid == 0 && l == 0) st4c(ws, WS_EP, tag);
  }
  BAR_ONLY();
  RING_ISSUE();
  WAIT_VM(3);
  if (l == 0 && lds_add1(L_CNT + 20) == 3) st4c(ws, WS_FU + bid * 64, tag);
  if (j == 1) GLDS_WORD(ws + WS_G + s0 * 4, l * 4, L_G);
  {
    const u32x4 em = {EMPTY, EMPTY, EMPTY, EMPTY};
    const unsigned eo = (l >> 4) * 1024 + (l & 15) * 16;
#pragma unroll
    for (int i = 0; i < 3; ++i) {
      const int gm = 10 * hg + (4 * i + j < 10 ? 4 * i + j : j);
      st16c(ws + WS_OF + qt * 81920 + (gm >> 2) * 4096 + (gm & 3) * 256, eo, em);
    }
    st16c(ws + WS_ZT + cha * 4096 + j * 2048, l * 16, em);
    st16c(ws + WS_ZT + cha * 4096 + j * 2048 + 1024, l * 16, em);
    if (l < 32) st16c(ws + WS_ZT + chs * 4096 + ((j & 1) ? 7 - js : js) * 512, l * 16, em);
  }
  BAR_ONLY();
  RING_ISSUE();
  {
    const bool neg = 2 * i16 - 30 + 2 * j < 0;
    if (l < 16) {
      LDS(unsigned, M_CV + j * CV_H + l * 4) = neg ? 0u : hv0;
      LDS(unsigned, M_CV - CV_CH + j * CV_H + l * 4) = neg ? 0u : hv1;
      LDS(unsigned, M_CV - 2 * CV_CH + j * CV_H + l * 4) = neg ? 0u : hv2;
    }
    if (j < 2 && l < 28) {
#pragma unroll
      for (int c = 0; c < 3; ++c) LDS(u32x4, M_CV - c * CV_CH + 4 * CV_H + j * CV_U + 4096 + l * 16) = u32x4{0, 0, 0, 0};
    }
  }
  WAIT_BAR(3);
  if (l == 0 && lds_add1(L_CNT + 16) == 3) st4c(ws, WS_FE + bid * 4, tag);
  f32x4 hi = ZERO4, lo = ZERO4;
#define WO_ISSUE(ks) GLDS5(a.wo + hg * 160 + (ks) * 32 * 1280, wo_off, w_at(ks) + 1024)
#define WE_ISSUE(e) GLDS5(a.wo + hg * 160 + ((e) < 7 ? (e) : (e) + 3) * 32 * 1280, wo_off, O_WE + (e) * 5120 + 1024)
#pragma unroll 1
  for (int bar = 3; bar < 16; ++bar) {
    if (next < 16) RING_ISSUE();
    if (bar < 6) {
      const int c = bar - 3;
      const char* hb = a.hk + (c == 0 ? chs : cha + c - 1) * 4096;
      GLDS3("", hb, ho0, M_CV - c * CV_CH + j * CV_H + 64);
      GLDS1("", hb + 3072, ho3, M_CV - c * CV_CH + j * CV_H + 64 + 3072);
      if (bar == 4 && j == 3) poll(ws, fu_off, tag);
      if (bar == 5) {
#pragma unroll
        for (int c2 = 0; c2 < 3; ++c2)
          GLDS2(" sc1", ws + WS_UT + (c2 == 0 ? chs : cha + c2 - 1) * 4096, uo,
                M_CV - c2 * CV_CH + 4 * CV_H + (j >> 1) * CV_U + (j & 1) * 2048);
      }
    } else if (bar == 6) {
      WAIT_VM(3);
      if (j == 1 && l == 63) {
#pragma unroll
        for (int c = 0; c < 3; ++c) LDS(u16, M_CV - c * CV_CH + 4 * CV_H + 4094) = 0;
      }
      WE_ISSUE(11 + j);
    } else if (bar < 13) {
      if (bar == 9 && !(j & 1)) {
        hi += LDS(const f32x4, M_RED + (j >> 1) * 2048 + l * 16);
        lo += LDS(const f32x4, M_RED + (j >> 1) * 2048 + 1024 + l * 16);
        conv_store(ws + WS_ZT + chs * 4096, cl, js, hi, lo);
      }
      if (bar == 10) WE_ISSUE(7 + j);
      if (bar == 11 && j < 2) WE_ISSUE(5 + j);
      const bool sh = bar < 9;
      conv_part(cl, sh ? js : j, sh ? 0 : 1, sh ? 4 * (j & 1) + 2 * (bar - 7) : 2 * (bar - 9), 2, sh ? dv0 : dv1, hi, lo);
      if (bar == 8 && (j & 1)) {
        LDS(f32x4, M_RED + (j >> 1) * 2048 + l * 16) = hi;
        LDS(f32x4, M_RED + (j >> 1) * 2048 + 1024 + l * 16) = lo;
      }
      if (bar == 12) conv_store(ws + WS_ZT + cha * 4096, cl, j, hi, lo);
    } else if (bar == 13) {
      WE_ISSUE(1 + j);
      if (j == 2) {
#pragma unroll 1
        for (int i = 0; i < SPIN_MAX; ++i) {
          const u32x4 f = ld16c_wait(ws, WS_FE + l * 16);
          if (__all(f[0] == tag && f[1] == tag && f[2] == tag && f[3] == tag)) break;
          __builtin_amdgcn_s_sleep(1);
        }
      }
    } else if (bar == 14) {
      if (j < 3) WE_ISSUE(j == 0 ? 0 : 14 + j);
    }
    if (bar < 14) {
      if (bar < 7) WAIT_BAR(7); else WAIT_BAR(3);
    } else {
      WAIT_BAR(0);
    }
  }
  STAMP(8);
#define CSPLIT 4
#define HP3 2
  __builtin_amdgcn_s_setprio(HP3);
  BAR_RAW();
  if (j < 3) WO_ISSUE(7 + j);
  STAMP(9);
#define HZ_AT 6
  conv_part(cl, j, 2, 0, HZ_AT, dv2, hi, lo);
  if (HZ_AT < 8) {
    if (j == 0 && l == 0) st4c(ws, WS_HZ + bid * 64, tag);
    conv_part(cl, j, 2, HZ_AT, 8 - HZ_AT, dv2, hi, lo);
  }
  WAIT_VM(0);
  conv_store(ws + WS_ZT + (cha + 1) * 4096, cl, j, hi, lo);
  if (l == 0 && lds_add1(L_CNT + 4) == 3 && HZ_AT == 8) st4c(ws, WS_HZ + bid * 64, tag);
  STAMP(10);
  if (j == 3) {
    poll(ws, WS_HZ + (32 * hg + (l & 31)) * 64, tag);
    LDS_WORD(24) = 1u;
  }
#pragma unroll 1
  for (int i = 0; i < SPIN_MAX * 16; ++i) {
    if (__builtin_amdgcn_readfirstlane(LDS_WORD(24)) != 0 && __builtin_amdgcn_readfirstlane(LDS_WORD(4)) == 4) break;
    __builtin_amdgcn_s_sleep(1);
  }
  {
    const char* b_ = ws + WS_ZT + (hg * 80 + 8 * j) * 4096 + s0 * 2;
    const char* b2 = b_ + (j < 2 ? 64 : 0) * 4096;
    const unsigned d2 = O_Z + j * 1024 + (j < 2 ? 8192 : 0);
#pragma unroll 1
    for (int i = 0; i < SPIN_MAX; ++i) {
      GLDS1(" sc1", b_, zo, O_Z + j * 1024);
      GLDS1(" sc1", b_ + 32 * 4096, zo, O_Z + j * 1024 + 4096);
      GLDS1(" sc1", b2, zo, d2);
      WAIT_VM(0);
      const u32x4 z0 = *(volatile LDS_AS u32x4*)(size_t)(O_Z + j * 1024 + l * 16);
      const u32x4 z1 = *(volatile LDS_AS u32x4*)(size_t)(O_Z + j * 1024 + 4096 + l * 16);
      const u32x4 z2 = *(volatile LDS_AS u32x4*)(size_t)(d2 + l * 16);
      unsigned m = umax3(umax3(z0[0], z0[1], z0[2]), z0[3], z1[0]);
      m = umax3(umax3(umax3(m, z1[1], z1[2]), z1[3], z2[0]), z2[1], umax3(z2[2], z2[3], 0u));
      if (!__any(m == EMPTY)) break;
    }
  }
  STAMP(11);
  BAR_RAW();
  STAMP(12);
  STAMP_FLUSH();
}

__global__ void __launch_bounds__(768) __attribute__((amdgpu_waves_per_eu(3, 3))) k_fused(Args a) {
  const int bid = blockIdx.x;
  const int qt = (bid & 7) * 4 + ((bid >> 3) & 3), hg = bid >> 5;
  const int l = threadIdx.x & 63;
  const int w = __builtin_amdgcn_readfirstlane(threadIdx.x >> 6);
  if (w < 4) help_wave(a, bid, qt, hg, w, l);
  else mat_wave(a, bid, qt, hg, w - 4, l);
}

extern "C" void kernel_launch(void* const* d_in, const int* in_sizes, int n_in, void* d_out, int out_size, void* d_ws,
                              size_t ws_size, hipStream_t stream) {
  Args a;
  a.x = (const char*)d_in[0];
  a.kin = (const char*)d_in[1];
  a.vin = (const char*)d_in[2];
  a.shk = (const char*)d_in[3];
  a.hk = (const char*)d_in[4];
  a.sd = (const char*)d_in[5];
  a.wq = (const char*)d_in[7];
  a.wk = (const char*)d_in[8];
  a.wv = (const char*)d_in[9];
  a.wo = (const char*)d_in[10];
  a.gw = (const char*)d_in[11];
  a.ws = (char*)d_ws;
  a.out = (char*)d_out;
  (void)hipFuncSetAttribute((const void*)k_fused, hipFuncAttributeMaxDynamicSharedMemorySize, L_BYTES);
  k_fused<<<256, 768, L_BYTES, stream>>>(a);
}
```
